```python
import torch

def _softmax(x, axis=-1):
    return torch.softmax(x, dim=axis)
def _gelu(x):
    return torch.nn.functional.gelu(x, approximate="tanh")
def _silu(x):
    return torch.nn.functional.silu(x)
def _sigmoid(x):
    return torch.sigmoid(x)
def _layernorm(x, axis=-1, eps=1e-5):
    m = x.mean(dim=axis, keepdim=True)
    v = x.var(dim=axis, keepdim=True, unbiased=False)
    return (x - m) / torch.sqrt(v + eps)
def _scatter_add(tgt, idx, src, axis=0):
    return tgt.clone().index_add_(axis, idx, src)
def _segment_sum(src, seg, n):
    out = torch.zeros((n,) + src.shape[1:], dtype=src.dtype)
    return out.index_add_(0, seg, src)
def _pad(x, pw):
    flat = tuple(v for pair in reversed(pw) for v in pair)
    return torch.nn.functional.pad(x, flat)

kernel_name = 'concat_heads__gqa_block_cached__gqa_block_cached__near_ridge'

def setup_inputs():

    _w = (lambda a: (lambda t: t.to(torch.bfloat16) if t.is_floating_point() else t)(torch.as_tensor(a)))
    def _A_setup():
        B, Sq, D = 8, 192, 640
        Hq, Hkv, Sk, Dh = 32, 32, 512, 192
        assert Hq % Hkv == 0, (Hq, Hkv)
        return {
            "x": _w((lambda *s: torch.randn(*s, dtype=torch.bfloat16) * 0.8)(B, Sq, D)),
            "k_cache": _w((lambda *s: torch.randn(*s, dtype=torch.bfloat16) * 0.8)(B, Hkv, Sk, Dh)),
            "v_cache": _w((lambda *s: torch.randn(*s, dtype=torch.bfloat16) * 0.05)(B, Hkv, Sk, Dh)),
            "wq": _w((lambda *s: torch.randn(*s, dtype=torch.bfloat16) * 0.8)(D, Hq * Dh) * (D ** -0.5)),
            "wo": _w((lambda *s: torch.randn(*s, dtype=torch.bfloat16) * 0.05)(Hq * Dh, D) * ((Hq * Dh) ** -0.5)),
        }
    _A_in = _A_setup()

    def _B_setup():
        B, Sq, D = 8, 192, 640
        Hq, Hkv, Sk, Dh = 32, 32, 512, 192
        assert Hq % Hkv == 0, (Hq, Hkv)
        return {
            "x": _w((lambda *s: torch.randn(*s, dtype=torch.bfloat16) * 0.8)(B, Sq, D)),
            "k_cache": _w((lambda *s: torch.randn(*s, dtype=torch.bfloat16) * 0.8)(B, Hkv, Sk, Dh)),
            "v_cache": _w((lambda *s: torch.randn(*s, dtype=torch.bfloat16) * 0.05)(B, Hkv, Sk, Dh)),
            "wq": _w((lambda *s: torch.randn(*s, dtype=torch.bfloat16) * 0.8)(D, Hq * Dh) * (D ** -0.5)),
            "wo": _w((lambda *s: torch.randn(*s, dtype=torch.bfloat16) * 0.05)(Hq * Dh, D) * ((Hq * Dh) ** -0.5)),
        }
    _B_in = _B_setup()

    return {
        "A_x": _A_in["x"],
        "A_k_cache": _A_in["k_cache"],
        "A_v_cache": _A_in["v_cache"],
        "A_wq": _A_in["wq"],
        "A_wo": _A_in["wo"],
        "B_x": _B_in["x"],
        "B_k_cache": _B_in["k_cache"],
        "B_v_cache": _B_in["v_cache"],
        "B_wq": _B_in["wq"],
        "B_wo": _B_in["wo"],
    }

def reference(A_x, A_k_cache, A_v_cache, A_wq, A_wo, B_x, B_k_cache, B_v_cache, B_wq, B_wo):
    def _A_ref(x, k_cache, v_cache, wq, wo):
        B, Sq, D = x.shape
        Hq, Dh = 32, 192
        Hkv = k_cache.shape[1]
        rep = Hq // Hkv
        q = (lambda x, *d: x.permute(*d))((x @ wq).reshape(B, Sq, Hq, Dh), 0, 2, 1, 3)
        k = (lambda x, n, axis: torch.repeat_interleave(x, n, dim=axis))(k_cache, rep, axis=1)
        v = (lambda x, n, axis: torch.repeat_interleave(x, n, dim=axis))(v_cache, rep, axis=1)
        scores = torch.einsum("bhqd,bhkd->bhqk", q, k) * (Dh ** -0.5)
        p = _softmax(scores, axis=-1)
        o = torch.einsum("bhqk,bhkd->bhqd", p, v)
        return (lambda x, *d: x.permute(*d))(o, 0, 2, 1, 3).reshape(B, Sq, Hq * Dh) @ wo

    def _B_ref(x, k_cache, v_cache, wq, wo):
        B, Sq, D = x.shape
        Hq, Dh = 32, 192
        Hkv = k_cache.shape[1]
        rep = Hq // Hkv
        q = (lambda x, *d: x.permute(*d))((x @ wq).reshape(B, Sq, Hq, Dh), 0, 2, 1, 3)
        k = (lambda x, n, axis: torch.repeat_interleave(x, n, dim=axis))(k_cache, rep, axis=1)
        v = (lambda x, n, axis: torch.repeat_interleave(x, n, dim=axis))(v_cache, rep, axis=1)
        scores = torch.einsum("bhqd,bhkd->bhqk", q, k) * (Dh ** -0.5)
        p = _softmax(scores, axis=-1)
        o = torch.einsum("bhqk,bhkd->bhqd", p, v)
        return (lambda x, *d: x.permute(*d))(o, 0, 2, 1, 3).reshape(B, Sq, Hq * Dh) @ wo

    a = _A_ref(A_x, A_k_cache, A_v_cache, A_wq, A_wo)
    b = _B_ref(B_x, B_k_cache, B_v_cache, B_wq, B_wo)
    return torch.concatenate([a, b], axis=-1)
```

```cpp
#include <hip/hip_runtime.h>
#include <hip/hip_bf16.h>
#include <stdint.h>
#include <type_traits>

#define DI __device__ __forceinline__
typedef unsigned short u16;
typedef __attribute__((ext_vector_type(8))) __bf16 bf16x8;
typedef __attribute__((ext_vector_type(2))) __bf16 bf16x2;
typedef __attribute__((ext_vector_type(4))) short s16x4;
typedef __attribute__((ext_vector_type(8))) short s16x8;
typedef __attribute__((ext_vector_type(4))) float f32x4;
typedef __attribute__((ext_vector_type(2))) float f32x2;
typedef __attribute__((ext_vector_type(4))) unsigned u32x4;
typedef __attribute__((address_space(3))) void lds_void;
typedef __attribute__((address_space(3))) s16x4 lds_s16x4;

constexpr int NB = 8, SQ = 192, DM = 640, NH = 32, SK = 512, DH = 192;
constexpr int HD = NH * DH;
constexpr int MROWS = NB * SQ;
constexpr int SLOT = 24576;
constexpr int K1_LDS = 160 * 1024;

DI unsigned cvtpk(float lo, float hi) {
  f32x2 v = {lo, hi};
  bf16x2 b = __builtin_convertvector(v, bf16x2);
  return __builtin_bit_cast(unsigned, b);
}
DI f32x4 mfma16(bf16x8 a, bf16x8 b, f32x4 c) { return __builtin_amdgcn_mfma_f32_16x16x32_bf16(a, b, c, 0, 0, 0); }
DI void mfma_a(f32x4& c, bf16x8 a, bf16x8 b) { asm volatile("v_mfma_f32_16x16x32_bf16 %0, %1, %2, %0" : "+a"(c) : "v"(a), "v"(b)); }
DI void mfma_v(f32x4& c, bf16x8 a, bf16x8 b) { asm volatile("v_mfma_f32_16x16x32_bf16 %0, %1, %2, %0" : "+v"(c) : "v"(a), "a"(b)); }
DI void mfma_v0(f32x4& d, bf16x8 a, bf16x8 b, f32x4 c) { asm volatile("v_mfma_f32_16x16x32_bf16 %0, %1, %2, %3" : "=&v"(d) : "v"(a), "a"(b), "v"(c)); }
DI float fadd_s(float a, float b) { float r = a + b; asm("" : "+v"(r)); return r; }
#define MFMA_DRAIN() asm volatile("s_nop 7\n\ts_nop 7" ::: "memory")
DI bf16x8 lds_b128(const char* p) { return *reinterpret_cast<const bf16x8*>(p); }
DI bf16x8 lds_tr2(const char* p0, const char* p1) {
  s16x4 lo = __builtin_amdgcn_ds_read_tr16_b64_v4i16((lds_s16x4*)p0);
  s16x4 hi = __builtin_amdgcn_ds_read_tr16_b64_v4i16((lds_s16x4*)p1);
  s16x8 v = __builtin_shufflevector(lo, hi, 0, 1, 2, 3, 4, 5, 6, 7);
  return __builtin_bit_cast(bf16x8, v);
}
DI s16x4 lds_tr(const char* p) { return __builtin_amdgcn_ds_read_tr16_b64_v4i16((lds_s16x4*)p); }
DI bf16x8 cat8(s16x4 lo, s16x4 hi) { s16x8 v = __builtin_shufflevector(lo, hi, 0, 1, 2, 3, 4, 5, 6, 7); return __builtin_bit_cast(bf16x8, v); }
#define SB() __builtin_amdgcn_sched_barrier(0)
#define WAITV(n) asm volatile("s_waitcnt vmcnt(" #n ")" ::: "memory")
#define STAMP() do {} while (0)
#define PHASE_SYNC(n) do { STAMP(); asm volatile("s_waitcnt vmcnt(" #n ") lgkmcnt(0)" ::: "memory"); STAMP(); __builtin_amdgcn_s_barrier(); asm volatile("" ::: "memory"); STAMP(); } while (0)

DI u32x4 pair_fix(unsigned a0, unsigned a1, unsigned b0, unsigned b1) {
  auto r0 = __builtin_amdgcn_permlane32_swap(a0, b0, false, false);
  auto r1 = __builtin_amdgcn_permlane32_swap(a1, b1, false, false);
  auto s0 = __builtin_amdgcn_permlane16_swap(r0[0], r0[1], false, false);
  auto s1 = __builtin_amdgcn_permlane16_swap(r1[0], r1[1], false, false);
  u32x4 o = {s0[0], s1[0], s0[1], s1[1]};
  return o;
}

struct DmaOff { unsigned k[6], v[6], w[6], x[6]; };

DI void dma_tile(__amdgpu_buffer_rsrc_t rs, const unsigned (&vo)[6], unsigned soff, char* smem, int slot, int w) {
  const unsigned lb = (unsigned)(size_t)smem + (unsigned)(slot * SLOT + w * 6144);
#pragma unroll
  for (int j = 0; j < 6; ++j)
    asm volatile("s_mov_b32 m0, %0\n\ts_nop 0\n\tbuffer_load_dwordx4 %1, %2, %3 offen lds" :: "s"(lb + j * 1024u), "v"(vo[j]), "s"(rs), "s"(soff) : "memory");
}

struct DmaJob { __amdgpu_buffer_rsrc_t rs; unsigned soff, lb; unsigned vo[6]; int valid; };
DI DmaJob mkjob(__amdgpu_buffer_rsrc_t rs, const unsigned (&vo)[6], unsigned soff, char* smem, int slot, int w) {
  DmaJob j; j.rs = rs; j.soff = soff; j.lb = (unsigned)(size_t)smem + (unsigned)(slot * SLOT + w * 6144); j.valid = 1;
#pragma unroll
  for (int i = 0; i < 6; ++i) j.vo[i] = vo[i];
  return j;
}
DI void fixjob(DmaJob& j) { j.soff = __builtin_amdgcn_readfirstlane(j.soff); j.lb = __builtin_amdgcn_readfirstlane(j.lb); }
DI void dma_piece(const DmaJob& jb, int j) {
  asm volatile("s_mov_b32 m0, %0\n\ts_nop 0\n\tbuffer_load_dwordx4 %1, %2, %3 offen lds" :: "s"(jb.lb + j * 1024u), "v"(jb.vo[j]), "s"(jb.rs), "s"(jb.soff) : "memory");
}

__global__ __launch_bounds__(256, 1) void k1_attn(const u16* __restrict__ xA, const u16* __restrict__ kA, const u16* __restrict__ vA, const u16* __restrict__ wqA,
                                                  const u16* __restrict__ xB, const u16* __restrict__ kB, const u16* __restrict__ vB, const u16* __restrict__ wqB,
                                                  u16* __restrict__ Og, unsigned* __restrict__ cnt) {
  extern __shared__ __attribute__((aligned(16))) char smem[];
  const int tid = threadIdx.x, l = tid & 63;
  const int w = __builtin_amdgcn_readfirstlane(tid >> 6);
  const int r = l & 15, g = l >> 4, q4 = (l & 15) >> 2, p4 = l & 3;
  const int wg = blockIdx.x, xcd = wg & 7, idx = wg >> 3;
  const int head = xcd * 4 + (idx & 3), b = idx >> 2;

  if (blockIdx.x == 0 && tid < 64) cnt[tid * 32] = 0u;
  DmaOff D;
#pragma unroll
  for (int j = 0; j < 6; ++j) {
    const int p = (w * 6 + j) * 64 + l;
    const int row = p / 24, chp = p % 24;
    D.k[j] = row * 384 + ((chp & ~7) | ((chp & 7) ^ ((row >> 1) & 7))) * 16;
    D.v[j] = row * 384 + ((chp & ~7) | ((chp & 7) ^ (((row >> 1) & 3) << 1))) * 16;
    D.w[j] = row * 12288 + ((chp & ~7) | ((chp & 7) ^ ((((row >> 1) & 1) << 1) | (((row >> 3) & 1) << 2)))) * 16;
    const int rx = p >> 3, cx = p & 7;
    D.x[j] = rx * 1280 + (cx ^ ((rx >> 1) & 7)) * 16;
  }
  const int kb0 = r * 384 + ((g) ^ (r >> 1)) * 16;
  const int kb1 = r * 384 + ((4 + g) ^ (r >> 1)) * 16;
  int vbs[4], wbs[4];
  {
    const int fv = ((2 * g + (q4 >> 1)) & 3) << 1;
    const int fw = ((q4 >> 1) << 1) | ((g & 1) << 2);
#pragma unroll
    for (int c = 0; c < 4; ++c) {
      vbs[c] = (4 * g + q4) * 384 + (((2 * c) ^ fv) + (p4 >> 1)) * 16 + 8 * (p4 & 1);
      wbs[c] = (8 * g + q4) * 384 + (((2 * c) ^ fw) + (p4 >> 1)) * 16 + 8 * (p4 & 1);
    }
  }
  const int xb0 = (48 * w + r) * 128 + ((g) ^ (r >> 1)) * 16;
  const int xb1 = (48 * w + r) * 128 + ((4 + g) ^ (r >> 1)) * 16;

  const unsigned xoff = (unsigned)b * (SQ * DM * 2);
  const unsigned woff = (unsigned)head * (DH * 2);
  const unsigned kvoff = (unsigned)(b * NH + head) * (SK * DH * 2);

  __amdgpu_buffer_rsrc_t rX = __builtin_amdgcn_make_buffer_rsrc((void*)xA, 0, NB * SQ * DM * 2, 0x00020000);
  __amdgpu_buffer_rsrc_t rW = __builtin_amdgcn_make_buffer_rsrc((void*)wqA, 0, DM * HD * 2, 0x00020000);
  __amdgpu_buffer_rsrc_t rK = __builtin_amdgcn_make_buffer_rsrc((void*)kA, 0, NB * NH * SK * DH * 2, 0x00020000);
  __amdgpu_buffer_rsrc_t rV = __builtin_amdgcn_make_buffer_rsrc((void*)vA, 0, NB * NH * SK * DH * 2, 0x00020000);
  const __amdgpu_buffer_rsrc_t rXn = __builtin_amdgcn_make_buffer_rsrc((void*)xB, 0, NB * SQ * DM * 2, 0x00020000);
  const __amdgpu_buffer_rsrc_t rWn = __builtin_amdgcn_make_buffer_rsrc((void*)wqB, 0, DM * HD * 2, 0x00020000);
  const __amdgpu_buffer_rsrc_t rKn = __builtin_amdgcn_make_buffer_rsrc((void*)kB, 0, NB * NH * SK * DH * 2, 0x00020000);
  const __amdgpu_buffer_rsrc_t rVn = __builtin_amdgcn_make_buffer_rsrc((void*)vB, 0, NB * NH * SK * DH * 2, 0x00020000);

#define ISSUE_X(R, i, slot) dma_tile(R, D.x, xoff + (i) * 128u, smem, slot, w)
#define ISSUE_W(R, i, slot) dma_tile(R, D.w, woff + (i) * (64u * 12288u), smem, slot, w)
#define ISSUE_K(t, slot) dma_tile(rK, D.k, kvoff + (t) * 24576u, smem, slot, w)
#define ISSUE_V(t, slot) dma_tile(rV, D.v, kvoff + (t) * 24576u, smem, slot, w)

#define JOB_X(R, i, slot) mkjob(R, D.x, xoff + (i) * 128u, smem, slot, w)
#define JOB_W(R, i, slot) mkjob(R, D.w, woff + (i) * (64u * 12288u), smem, slot, w)
#define JOB_K(t, slot) mkjob(rK, D.k, kvoff + (t) * 24576u, smem, slot, w)
#define JOB_V(t, slot) mkjob(rV, D.v, kvoff + (t) * 24576u, smem, slot, w)
  DmaJob DUMMY = JOB_X(rX, 0, 0); DUMMY.lb = (unsigned)(size_t)smem + 6u * SLOT + (unsigned)w * 1024u;

  ISSUE_X(rX, 0, 0); ISSUE_W(rW, 0, 1); ISSUE_X(rX, 1, 2); ISSUE_W(rW, 1, 3); ISSUE_X(rX, 2, 4); ISSUE_W(rW, 2, 5);

  const float QSCALE = 0.07216878364870322f * 1.4426950408889634f;
  constexpr std::true_type T_{}; constexpr std::false_type F_{};

  for (int unit = 0; unit < 2; ++unit) {
    f32x4 acc[12][3];
#pragma unroll
    for (int c = 0; c < 12; ++c)
#pragma unroll
      for (int qt = 0; qt < 3; ++qt) { acc[c][qt] = f32x4{0.f, 0.f, 0.f, 0.f}; asm volatile("" : "+a"(acc[c][qt])); }

    {
      bf16x8 xf[2][3];
      s16x4 wlo[4], whi[4];
      auto waddr = [&](const char* pw, int i) { const int ks1 = i / 12, c1 = i % 12; return pw + wbs[c1 & 3] + (32 * ks1) * 384 + (c1 >> 2) * 128; };
      auto qphase = [&](int sx, int sxn, const DmaJob& j0, const DmaJob& j1, auto hasNext_, int syncn) {
        constexpr bool hasNext = decltype(hasNext_)::value;
        const char* px = smem + sx * SLOT;
        const char* pw = px + SLOT;
        const char* pxn = smem + sxn * SLOT;
        const char* pwn = pxn + SLOT;
#pragma unroll
        for (int i = 0; i < 24; ++i) {
          const int ks = i / 12, c = i % 12;
          const bf16x8 wf = cat8(wlo[i & 3], whi[i & 3]);
          if constexpr (hasNext) {
            if (i == 21) { if (syncn == 12) { PHASE_SYNC(12); } else if (syncn == 18) { PHASE_SYNC(18); } else { PHASE_SYNC(24); } }
          }
          const bool ld = (i + 3 < 24) || hasNext;
          const char* an = (i + 3 < 24) ? waddr(pw, i + 3) : waddr(pwn, i + 3 - 24);
          mfma_a(acc[c][0], wf, xf[ks][0]);
          if (ld) wlo[(i + 3) & 3] = lds_tr(an);
          SB();
          mfma_a(acc[c][1], wf, xf[ks][1]);
          if (ld) whi[(i + 3) & 3] = lds_tr(an + 4 * 384);
          SB();
          mfma_a(acc[c][2], wf, xf[ks][2]);
          if (i < 3) xf[1][i] = lds_b128(px + xb1 + i * 2048);
          else if (i >= 21) { if constexpr (hasNext) xf[0][i - 21] = lds_b128(pxn + xb0 + (i - 21) * 2048); }
          else if (i < 15) { if (i & 1) dma_piece(j0, (i - 3) >> 1); }
          else dma_piece(j1, i - 15);
          SB();
        }
      };
      if (unit == 0) { PHASE_SYNC(24); } else { PHASE_SYNC(36); }
#pragma unroll
      for (int i = 0; i < 3; ++i) xf[0][i] = lds_b128(smem + xb0 + i * 2048);
#pragma unroll
      for (int i = 0; i < 3; ++i) { const char* a = waddr(smem + SLOT, i); wlo[i] = lds_tr(a); whi[i] = lds_tr(a + 4 * 384); }
      int sx = 0;
      for (int i = 0; i < 9; ++i) {
        const int sp = (sx == 0) ? 4 : sx - 2;
        const int sxn = (sx == 4) ? 0 : sx + 2;
        DmaJob j0, j1;
        if (i == 0) { j0 = JOB_W(rW, 2, 5); if (unit == 0) j0 = DUMMY; j1 = DUMMY; }
        else if (i < 8) { j0 = JOB_X(rX, i + 2, sp); j1 = JOB_W(rW, i + 2, sp + 1); }
        else { j0 = JOB_K(0, 2); j1 = JOB_K(1, 3); }
        fixjob(j0); fixjob(j1);
        qphase(sx, sxn, j0, j1, T_, i == 0 ? 24 : (i == 1 ? 18 : 12));
        sx = sxn;
      }
      qphase(0, 0, JOB_V(0, 4), JOB_K(2, 5), F_, 0);
    }

    MFMA_DRAIN();
    bf16x8 qf[3][6];
#pragma unroll
    for (int qt = 0; qt < 3; ++qt)
#pragma unroll
      for (int s = 0; s < 6; ++s) {
        asm volatile("" : "+a"(acc[2 * s][qt]), "+a"(acc[2 * s + 1][qt]));
        const f32x4 A = acc[2 * s][qt] * QSCALE, B = acc[2 * s + 1][qt] * QSCALE;
        u32x4 o = pair_fix(cvtpk(A[0], A[1]), cvtpk(A[2], A[3]), cvtpk(B[0], B[1]), cvtpk(B[2], B[3]));
        qf[qt][s] = __builtin_bit_cast(bf16x8, o);
        asm volatile("" : "+a"(qf[qt][s]));
        SB();
      }
#pragma unroll
    for (int c = 0; c < 12; ++c)
#pragma unroll
      for (int qt = 0; qt < 3; ++qt) { acc[c][qt] = f32x4{0.f, 0.f, 0.f, 0.f}; asm volatile("" : "+a"(acc[c][qt])); }

    float mref[3], lsum[3] = {0.f, 0.f, 0.f};
    f32x4 negm[3] = {f32x4{0.f, 0.f, 0.f, 0.f}, f32x4{0.f, 0.f, 0.f, 0.f}, f32x4{0.f, 0.f, 0.f, 0.f}};
    f32x4 Sc[2][3];
    bf16x8 Pp[3];
    bf16x8 kf[3];
    s16x4 vlo[3], vhi[3];

    auto sm = [&](const f32x4 (&S)[2][3], bf16x8 (&P)[3], float (&ps)[3]) {
#pragma unroll
      for (int qt = 0; qt < 3; ++qt) {
        float e[8];
#pragma unroll
        for (int i = 0; i < 8; ++i) e[i] = __builtin_amdgcn_exp2f(S[i >> 2][qt][i & 3]);
        ps[qt] = ((e[0] + e[1]) + (e[2] + e[3])) + ((e[4] + e[5]) + (e[6] + e[7]));
        u32x4 o = {cvtpk(e[0], e[1]), cvtpk(e[2], e[3]), cvtpk(e[4], e[5]), cvtpk(e[6], e[7])};
        P[qt] = __builtin_bit_cast(bf16x8, o);
      }
    };
    auto rowmax = [&](const f32x4 (&S)[2][3], int qt) {
      float mx = fmaxf(fmaxf(fmaxf(S[0][qt][0], S[0][qt][1]), fmaxf(S[0][qt][2], S[0][qt][3])),
                       fmaxf(fmaxf(S[1][qt][0], S[1][qt][1]), fmaxf(S[1][qt][2], S[1][qt][3])));
      mx = fmaxf(mx, __shfl_xor(mx, 16));
      mx = fmaxf(mx, __shfl_xor(mx, 32));
      return mx;
    };
    auto kaddr = [&](const char* pk, int i) { const int s1 = i >> 1, k21 = i & 1; return pk + ((s1 & 1) ? kb1 : kb0) + k21 * 6144 + (s1 >> 1) * 128; };
    auto vaddr = [&](const char* pv, int c1) { return pv + vbs[c1 & 3] + (c1 >> 2) * 128; };
    auto kbase = [&](int slot, int hb) { return (const char*)smem + slot * SLOT + hb * (2 * 6144); };
    auto vbase = [&](int slot, int ks) { return (const char*)smem + slot * SLOT + (32 * ks) * 384; };
    auto step = [&](auto doQK_, const char* pk, auto doPV_, const char* pvv, auto doSM_, const DmaJob& jb,
                    auto nQK_, const char* pkn, auto nPV_, const char* pvn, auto hasSync_, int syncn) {
      constexpr bool doQK = decltype(doQK_)::value, doPV = decltype(doPV_)::value, doSM = decltype(doSM_)::value;
      constexpr bool nQK = decltype(nQK_)::value, nPV = decltype(nPV_)::value, hasSync = decltype(hasSync_)::value;
      f32x4 Sn[2][3];
      u32x4 Pn[3];
      float ps[3] = {0.f, 0.f, 0.f};
      float ea = 0.f, eb = 0.f;
#pragma unroll
      for (int i = 0; i < 12; ++i) {
        const int s = i >> 1, k2 = i & 1, ri = i % 3, rn = (i + 2) % 3;
        if constexpr (hasSync) { if (i == 10) { if (syncn == 12) { PHASE_SYNC(12); } else { PHASE_SYNC(18); } } }
        const bool in = (i + 2 < 12);
        const bool ldK = in ? doQK : nQK, ldV = in ? doPV : nPV;
        const char* ka = in ? kaddr(pk, i + 2) : kaddr(pkn, i + 2 - 12);
        const char* va = in ? vaddr(pvv, i + 2) : vaddr(pvn, i + 2 - 12);
        const bf16x8 kc = kf[ri];
        const bf16x8 vc = cat8(vlo[ri], vhi[ri]);
        const int pq = (i - 1) >> 2, pj = (i - 1) & 3;
        const int eq = i >> 2, e0 = (i & 3) * 2;
        if constexpr (doQK) { if (s == 0) mfma_v0(Sn[k2][0], kc, qf[0][s], negm[0]); else mfma_v(Sn[k2][0], kc, qf[0][s]); }
        if (ldV) vlo[rn] = lds_tr(va);
        if constexpr (doSM) { if (i > 0) { unsigned pk2 = cvtpk(ea, eb); asm volatile("" : "+v"(pk2)); Pn[pq][pj] = pk2; } }
        SB();
        if constexpr (doQK) { if (s == 0) mfma_v0(Sn[k2][1], kc, qf[1][s], negm[1]); else mfma_v(Sn[k2][1], kc, qf[1][s]); }
        if (ldV) vhi[rn] = lds_tr(va + 16 * 384);
        if constexpr (doSM) { if (i > 0) { if (pj == 0) ps[pq] = fadd_s(ea, eb); else ps[pq] = fadd_s(ps[pq], ea); } }
        SB();
        if constexpr (doQK) { if (s == 0) mfma_v0(Sn[k2][2], kc, qf[2][s], negm[2]); else mfma_v(Sn[k2][2], kc, qf[2][s]); }
        if (ldK) kf[rn] = lds_b128(ka);
        if constexpr (doSM) { if (i > 0) { if (pj != 0) ps[pq] = fadd_s(ps[pq], eb); } }
        SB();
        if constexpr (doPV) mfma_a(acc[i][0], vc, Pp[0]);
        if constexpr (doSM) { ea = __builtin_amdgcn_exp2f(Sc[e0 >> 2][eq][e0 & 3]); asm volatile("" : "+v"(ea)); }
        SB();
        if constexpr (doPV) mfma_a(acc[i][1], vc, Pp[1]);
        if constexpr (doSM) { eb = __builtin_amdgcn_exp2f(Sc[e0 >> 2][eq][(e0 & 3) + 1]); asm volatile("" : "+v"(eb)); }
        SB();
        if constexpr (doPV) mfma_a(acc[i][2], vc, Pp[2]);
        if (i < 10) { if ((i & 1) == 0) dma_piece(jb, i >> 1); else if (i == 9) dma_piece(jb, 5); }
        if constexpr (doQK) { if (i == 2) asm volatile("" :: "v"(negm[0]), "v"(negm[1]), "v"(negm[2])); }
        SB();
      }
      if constexpr (doSM) {
        Pn[2][3] = cvtpk(ea, eb);
        ps[2] = fadd_s(ps[2], ea); ps[2] = fadd_s(ps[2], eb);
        const bool bad = !(ps[0] <= 1.0e12f) || !(ps[1] <= 1.0e12f) || !(ps[2] <= 1.0e12f);
        bf16x8 Pb[3];
#pragma unroll
        for (int qt = 0; qt < 3; ++qt) Pb[qt] = __builtin_bit_cast(bf16x8, Pn[qt]);
        if (__builtin_expect(__any(bad), 0)) {
          MFMA_DRAIN();
          if constexpr (doQK) asm volatile("" : "+v"(Sn[0][0]), "+v"(Sn[0][1]), "+v"(Sn[0][2]), "+v"(Sn[1][0]), "+v"(Sn[1][1]), "+v"(Sn[1][2]));
#pragma unroll
          for (int qt = 0; qt < 3; ++qt) {
            const float d = fmaxf(rowmax(Sc, qt), 0.f);
            const float f = __builtin_amdgcn_exp2f(-d);
            mref[qt] += d;
            negm[qt] = f32x4{-mref[qt], -mref[qt], -mref[qt], -mref[qt]};
            asm volatile("s_nop 1" : "+v"(negm[qt]));
            lsum[qt] *= f;
#pragma unroll
            for (int c = 0; c < 12; ++c) { asm volatile("" : "+a"(acc[c][qt])); acc[c][qt] *= f; asm volatile("" : "+a"(acc[c][qt])); SB(); }
#pragma unroll
            for (int k2 = 0; k2 < 2; ++k2) { Sc[k2][qt] -= d; if constexpr (doQK) Sn[k2][qt] -= d; }
          }
          sm(Sc, Pb, ps);
        }
#pragma unroll
        for (int qt = 0; qt < 3; ++qt) { lsum[qt] += ps[qt]; Pp[qt] = Pb[qt]; asm volatile("s_nop 0" : "+v"(Pp[qt])); }
      }
      if constexpr (doQK) {
#pragma unroll
        for (int qt = 0; qt < 3; ++qt) { Sc[0][qt] = Sn[0][qt]; Sc[1][qt] = Sn[1][qt]; }
      }
    };

    PHASE_SYNC(18);
    kf[0] = lds_b128(kaddr(kbase(2, 0), 0)); kf[1] = lds_b128(kaddr(kbase(2, 0), 1));
    step(T_, kbase(2, 0), F_, smem, F_, JOB_V(1, 0), T_, kbase(2, 1), F_, smem, F_, 0);
    asm volatile("s_nop 7\n\ts_nop 7" : "+v"(Sc[0][0]), "+v"(Sc[0][1]), "+v"(Sc[0][2]), "+v"(Sc[1][0]), "+v"(Sc[1][1]), "+v"(Sc[1][2]));
#pragma unroll
    for (int qt = 0; qt < 3; ++qt) {
      const float mx = rowmax(Sc, qt);
      mref[qt] = mx;
      negm[qt] = f32x4{-mx, -mx, -mx, -mx};
      asm volatile("s_nop 1" : "+v"(negm[qt]));
      Sc[0][qt] -= mx; Sc[1][qt] -= mx;
    }
    step(T_, kbase(2, 1), F_, smem, T_, JOB_K(3, 1), T_, kbase(3, 0), T_, vbase(4, 0), T_, 18);
    {
      int ksl = 3;
      for (int t = 1; t < 7; ++t) {
        const int vsl = (ksl == 5) ? 0 : ksl + 1;
        const int kfree = (ksl == 1) ? 5 : ksl - 2;
        const int vfree = (kfree == 5) ? 0 : kfree + 1;
        const int ksn = (ksl == 5) ? 1 : ksl + 2, vsn = (ksn == 5) ? 0 : ksn + 1;
        DmaJob j0, j1;
        if (t == 1) { j0 = JOB_V(2, 2); j1 = DUMMY; }
        else if (t < 6) { j0 = JOB_K(t + 2, kfree); j1 = JOB_V(t + 1, vfree); }
        else { j0 = JOB_V(7, 5); j1 = JOB_X(rXn, 0, 0); if (unit) j1 = DUMMY; }
        fixjob(j0); fixjob(j1);
        step(T_, kbase(ksl, 0), T_, vbase(vsl, 0), T_, j0, T_, kbase(ksl, 1), T_, vbase(vsl, 1), F_, 0);
        step(T_, kbase(ksl, 1), T_, vbase(vsl, 1), T_, j1, T_, kbase(ksn, 0), T_, vbase(vsn, 0), T_, t < 3 ? 18 : 12);
        ksl = ksn;
      }
    }
    {
      DmaJob j0 = JOB_W(rWn, 0, 1), j1 = JOB_X(rXn, 1, 2), j2 = JOB_W(rWn, 1, 3), j3 = JOB_X(rXn, 2, 4);
      if (unit) { j0 = DUMMY; j1 = DUMMY; j2 = DUMMY; j3 = DUMMY; }
      fixjob(j0); fixjob(j1); fixjob(j2); fixjob(j3);
      step(T_, kbase(3, 0), T_, vbase(4, 0), T_, j0, T_, kbase(3, 1), T_, vbase(4, 1), F_, 0);
      step(T_, kbase(3, 1), T_, vbase(4, 1), T_, j1, F_, smem, T_, vbase(5, 0), T_, 18);
      step(F_, smem, T_, vbase(5, 0), T_, j2, F_, smem, T_, vbase(5, 1), F_, 0);
      step(F_, smem, T_, vbase(5, 1), F_, j3, F_, smem, F_, smem, F_, 0);
    }

    MFMA_DRAIN();
    {
      u16* orow = Og + (size_t)unit * MROWS * HD + (size_t)(b * SQ + 48 * w + r) * HD + head * DH + 8 * g;
#pragma unroll
      for (int qt = 0; qt < 3; ++qt) {
        float lt = lsum[qt];
        lt += __shfl_xor(lt, 16);
        lt += __shfl_xor(lt, 32);
        const float inv = 1.0f / lt;
#pragma unroll
        for (int s = 0; s < 6; ++s) {
          asm volatile("" : "+a"(acc[2 * s][qt]), "+a"(acc[2 * s + 1][qt]));
          const f32x4 A = acc[2 * s][qt] * inv, B = acc[2 * s + 1][qt] * inv;
          u32x4 o = pair_fix(cvtpk(A[0], A[1]), cvtpk(A[2], A[3]), cvtpk(B[0], B[1]), cvtpk(B[2], B[3]));
          *reinterpret_cast<u32x4*>(orow + (size_t)qt * 16 * HD + 32 * s) = o;
          SB();
        }
      }
    }
    STAMP();
    rX = rXn; rW = rWn; rK = rKn; rV = rVn;
  }
  asm volatile("s_waitcnt vmcnt(0)" ::: "memory");
}

constexpr int K2_STAGE = 24576 + 20480;
constexpr int K2_LDS = 3 * K2_STAGE;
constexpr size_t WS_O_BYTES = (size_t)2 * MROWS * HD * 2;
constexpr size_t WS_PART_OFF = WS_O_BYTES;
constexpr size_t WS_PART_BYTES = (size_t)64 * 4 * 8 * 15360;
constexpr size_t WS_CNT_OFF = WS_PART_OFF + WS_PART_BYTES;

__global__ __launch_bounds__(512, 2) void k2_oproj(const u16* __restrict__ Og, const u16* __restrict__ woA, const u16* __restrict__ woB,
                                                   u16* __restrict__ out, float* __restrict__ part, unsigned* __restrict__ cnt) {
  extern __shared__ __attribute__((aligned(16))) char smem[];
  const int tid = threadIdx.x, l = tid & 63;
  const int w = __builtin_amdgcn_readfirstlane(tid >> 6);
  const int r = l & 15, g = l >> 4, q4 = (l & 15) >> 2, p4 = l & 3;
  const int mw = w >> 1, nw = w & 1;
  const int wg = blockIdx.x, xcd = wg & 7, idx = wg >> 3;
  const int sk = idx & 3, tl = (idx >> 2) * 8 + xcd;
  const int br = tl >> 5, mt = (tl >> 2) & 7, nt = tl & 3;
  const u16* wo = br ? woB : woA;
  const __amdgpu_buffer_rsrc_t rO = __builtin_amdgcn_make_buffer_rsrc((void*)(Og + (size_t)br * MROWS * HD), 0, MROWS * HD * 2, 0x00020000);
  const __amdgpu_buffer_rsrc_t rW = __builtin_amdgcn_make_buffer_rsrc((void*)wo, 0, HD * DM * 2, 0x00020000);
  unsigned vo[3], vw[3], lo_[3], lw_[3];
#pragma unroll
  for (int j = 0; j < 3; ++j) {
    const int pi = w + 8 * j;
    const int p = pi * 64 + l, rx = p >> 3, cx = p & 7;
    vo[j] = rx * (HD * 2) + (cx ^ ((rx >> 1) & 7)) * 16;
    lo_[j] = pi * 1024;
    int wi = w + 8 * j; if (wi >= 20) wi -= 4;
    const int pw = wi * 64 + l, row = pw / 20, ch = pw % 20;
    vw[j] = row * (DM * 2) + (ch ^ (((row >> 3) & 1) << 1)) * 16;
    lw_[j] = 24576 + wi * 1024;
  }
  const unsigned ooff = (unsigned)(mt * 192) * (HD * 2) + (unsigned)sk * (1536 * 2);
  const unsigned woff = (unsigned)(sk * 1536) * (DM * 2) + (unsigned)nt * 320;
  const unsigned lbase = (unsigned)(size_t)smem;
  auto issue = [&](int st, int buf) {
    const unsigned lb = lbase + buf * K2_STAGE;
#pragma unroll
    for (int j = 0; j < 3; ++j) {
      asm volatile("s_mov_b32 m0, %0\n\ts_nop 0\n\tbuffer_load_dwordx4 %1, %2, %3 offen lds" :: "s"(lb + lo_[j]), "v"(vo[j]), "s"(rO), "s"(ooff + st * 128u) : "memory");
      asm volatile("s_mov_b32 m0, %0\n\ts_nop 0\n\tbuffer_load_dwordx4 %1, %2, %3 offen lds" :: "s"(lb + lw_[j]), "v"(vw[j]), "s"(rW), "s"(woff + st * (64u * DM * 2)) : "memory");
    }
  };
  const int ob0 = (48 * mw + r) * 128 + ((g) ^ (r >> 1)) * 16;
  const int ob1 = (48 * mw + r) * 128 + ((4 + g) ^ (r >> 1)) * 16;
  int wb[2];
  wb[0] = 24576 + (8 * g + q4) * 320 + (p4 >> 1) * 16 + 8 * (p4 & 1);
  f32x4 acc[5][3] = {};
  issue(0, 0); issue(1, 1);
  int buf = 0;
  for (int st = 0; st < 24; ++st) {
    if (st < 23) { asm volatile("s_waitcnt vmcnt(6) lgkmcnt(0)" ::: "memory"); } else { asm volatile("s_waitcnt vmcnt(0) lgkmcnt(0)" ::: "memory"); }
    __builtin_amdgcn_s_barrier();
    asm volatile("" ::: "memory");
    if (st + 2 < 24) issue(st + 2, buf == 0 ? 2 : buf - 1);
    const char* pb = smem + buf * K2_STAGE;
#pragma unroll
    for (int ks = 0; ks < 2; ++ks) {
      bf16x8 of[3];
#pragma unroll
      for (int m = 0; m < 3; ++m) of[m] = lds_b128(pb + (ks ? ob1 : ob0) + m * 2048);
#pragma unroll
      for (int c = 0; c < 5; ++c) {
        const char* a0 = pb + wb[0] + (32 * ks) * 320 + (((5 * nw + c) ^ (g & 1)) * 32);
        const bf16x8 wf = cat8(lds_tr(a0), lds_tr(a0 + 4 * 320));
#pragma unroll
        for (int m = 0; m < 3; ++m) acc[c][m] = mfma16(wf, of[m], acc[c][m]);
      }
    }
    buf = (buf == 2) ? 0 : buf + 1;
  }
  float* slab = part + ((size_t)(tl * 4) * 8) * 3840;
  const __amdgpu_buffer_rsrc_t rP = __builtin_amdgcn_make_buffer_rsrc((void*)slab, 0, 4 * 8 * 15360, 0x00020000);
  if (mw != sk) {
#pragma unroll
    for (int c = 0; c < 5; ++c)
#pragma unroll
      for (int m = 0; m < 3; ++m)
        __builtin_amdgcn_raw_buffer_store_b128(__builtin_bit_cast(u32x4, acc[c][m]), rP, l * 16, (unsigned)((sk * 8 + w) * 15360 + (c * 3 + m) * 1024), 16);
  }
  asm volatile("s_waitcnt vmcnt(0)" ::: "memory");
  __syncthreads();
  if (tid == 0) __hip_atomic_fetch_add(cnt + tl * 32, 1u, __ATOMIC_RELAXED, __HIP_MEMORY_SCOPE_AGENT);
  if (mw != sk) return;
  {
    unsigned spins = 0;
    while (__hip_atomic_load(cnt + tl * 32, __ATOMIC_RELAXED, __HIP_MEMORY_SCOPE_AGENT) < 4u) { __builtin_amdgcn_s_sleep(2); if (++spins > (1u << 24)) break; }
    asm volatile("" ::: "memory");
  }
#pragma unroll
  for (int s2 = 0; s2 < 4; ++s2) {
    if (s2 != sk) {
      u32x4 v[15];
#pragma unroll
      for (int i = 0; i < 15; ++i) v[i] = __builtin_amdgcn_raw_buffer_load_b128(rP, l * 16, (unsigned)((s2 * 8 + w) * 15360 + i * 1024), 16);
#pragma unroll
      for (int i = 0; i < 15; ++i) acc[i / 3][i % 3] += __builtin_bit_cast(f32x4, v[i]);
    }
  }
  u16* orow = out + (size_t)(mt * 192 + 48 * mw + r) * (2 * DM) + br * DM + nt * 160 + 80 * nw;
#pragma unroll
  for (int m = 0; m < 3; ++m) {
#pragma unroll
    for (int c = 0; c < 4; c += 2) {
      const f32x4 A = acc[c][m], B = acc[c + 1][m];
      u32x4 o = pair_fix(cvtpk(A[0], A[1]), cvtpk(A[2], A[3]), cvtpk(B[0], B[1]), cvtpk(B[2], B[3]));
      *reinterpret_cast<u32x4*>(orow + (size_t)m * 16 * (2 * DM) + 16 * c + 8 * g) = o;
    }
    const f32x4 E = acc[4][m];
    uint2 e2 = make_uint2(cvtpk(E[0], E[1]), cvtpk(E[2], E[3]));
    *reinterpret_cast<uint2*>(orow + (size_t)m * 16 * (2 * DM) + 64 + 4 * g) = e2;
  }
}

extern "C" void kernel_launch(void* const* d_in, const int* in_sizes, int n_in,
                              void* d_out, int out_size, void* d_ws, size_t ws_size,
                              hipStream_t stream) {
  const u16* xA = (const u16*)d_in[0]; const u16* kA = (const u16*)d_in[1]; const u16* vA = (const u16*)d_in[2];
  const u16* wqA = (const u16*)d_in[3]; const u16* woA = (const u16*)d_in[4];
  const u16* xB = (const u16*)d_in[5]; const u16* kB = (const u16*)d_in[6]; const u16* vB = (const u16*)d_in[7];
  const u16* wqB = (const u16*)d_in[8]; const u16* woB = (const u16*)d_in[9];
  u16* Og = (u16*)d_ws;
  float* part = (float*)((char*)d_ws + WS_PART_OFF);
  unsigned* cnt = (unsigned*)((char*)d_ws + WS_CNT_OFF);
  (void)hipFuncSetAttribute((const void*)k1_attn, hipFuncAttributeMaxDynamicSharedMemorySize, K1_LDS);
  (void)hipFuncSetAttribute((const void*)k2_oproj, hipFuncAttributeMaxDynamicSharedMemorySize, K2_LDS);
  hipLaunchKernelGGL(k1_attn, dim3(256), dim3(256), K1_LDS, stream, xA, kA, vA, wqA, xB, kB, vB, wqB, Og, cnt);
  hipLaunchKernelGGL(k2_oproj, dim3(256), dim3(512), K2_LDS, stream, Og, woA, woB, (u16*)d_out, part, cnt);
}
```

```cpp
#include <hip/hip_runtime.h>
#include <hip/hip_bf16.h>
#include <stdint.h>
#include <type_traits>

#define DI __device__ __forceinline__
typedef unsigned short u16;
typedef __attribute__((ext_vector_type(8))) __bf16 bf16x8;
typedef __attribute__((ext_vector_type(2))) __bf16 bf16x2;
typedef __attribute__((ext_vector_type(4))) short s16x4;
typedef __attribute__((ext_vector_type(8))) short s16x8;
typedef __attribute__((ext_vector_type(4))) float f32x4;
typedef __attribute__((ext_vector_type(2))) float f32x2;
typedef __attribute__((ext_vector_type(4))) unsigned u32x4;
typedef __attribute__((address_space(3))) void lds_void;
typedef __attribute__((address_space(3))) s16x4 lds_s16x4;

constexpr int NB = 8, SQ = 192, DM = 640, NH = 32, SK = 512, DH = 192;
constexpr int HD = NH * DH;
constexpr int MROWS = NB * SQ;
constexpr int SLOT = 24576;
constexpr int K1_LDS = 160 * 1024;

DI unsigned cvtpk(float lo, float hi) {
  f32x2 v = {lo, hi};
  bf16x2 b = __builtin_convertvector(v, bf16x2);
  return __builtin_bit_cast(unsigned, b);
}
DI f32x4 mfma16(bf16x8 a, bf16x8 b, f32x4 c) { return __builtin_amdgcn_mfma_f32_16x16x32_bf16(a, b, c, 0, 0, 0); }
DI void mfma_a(f32x4& c, bf16x8 a, bf16x8 b) { asm volatile("v_mfma_f32_16x16x32_bf16 %0, %1, %2, %0" : "+a"(c) : "v"(a), "v"(b)); }
DI void mfma_v(f32x4& c, bf16x8 a, bf16x8 b) { asm volatile("v_mfma_f32_16x16x32_bf16 %0, %1, %2, %0" : "+v"(c) : "v"(a), "a"(b)); }
DI void mfma_v0(f32x4& d, bf16x8 a, bf16x8 b, f32x4 c) { asm volatile("v_mfma_f32_16x16x32_bf16 %0, %1, %2, %3" : "=&v"(d) : "v"(a), "a"(b), "v"(c)); }
DI float fadd_s(float a, float b) { float r = a + b; asm("" : "+v"(r)); return r; }
#define MFMA_DRAIN() asm volatile("s_nop 7\n\ts_nop 7" ::: "memory")
DI bf16x8 lds_b128(const char* p) { return *reinterpret_cast<const bf16x8*>(p); }
DI bf16x8 lds_tr2(const char* p0, const char* p1) {
  s16x4 lo = __builtin_amdgcn_ds_read_tr16_b64_v4i16((lds_s16x4*)p0);
  s16x4 hi = __builtin_amdgcn_ds_read_tr16_b64_v4i16((lds_s16x4*)p1);
  s16x8 v = __builtin_shufflevector(lo, hi, 0, 1, 2, 3, 4, 5, 6, 7);
  return __builtin_bit_cast(bf16x8, v);
}
DI s16x4 lds_tr(const char* p) { return __builtin_amdgcn_ds_read_tr16_b64_v4i16((lds_s16x4*)p); }
DI bf16x8 cat8(s16x4 lo, s16x4 hi) { s16x8 v = __builtin_shufflevector(lo, hi, 0, 1, 2, 3, 4, 5, 6, 7); return __builtin_bit_cast(bf16x8, v); }
#define SB() __builtin_amdgcn_sched_barrier(0)
#define WAITV(n) asm volatile("s_waitcnt vmcnt(" #n ")" ::: "memory")
#define STAMP() do {} while (0)
#define PHASE_SYNC(n) do { STAMP(); asm volatile("s_waitcnt vmcnt(" #n ") lgkmcnt(0)" ::: "memory"); STAMP(); __builtin_amdgcn_s_barrier(); asm volatile("" ::: "memory"); STAMP(); } while (0)

DI u32x4 pair_fix(unsigned a0, unsigned a1, unsigned b0, unsigned b1) {
  auto r0 = __builtin_amdgcn_permlane32_swap(a0, b0, false, false);
  auto r1 = __builtin_amdgcn_permlane32_swap(a1, b1, false, false);
  auto s0 = __builtin_amdgcn_permlane16_swap(r0[0], r0[1], false, false);
  auto s1 = __builtin_amdgcn_permlane16_swap(r1[0], r1[1], false, false);
  u32x4 o = {s0[0], s1[0], s0[1], s1[1]};
  return o;
}

struct DmaOff { unsigned k[6], v[6], w[6], x[6]; };

DI void dma_tile(__amdgpu_buffer_rsrc_t rs, const unsigned (&vo)[6], unsigned soff, char* smem, int slot, int w) {
  const unsigned lb = (unsigned)(size_t)smem + (unsigned)(slot * SLOT + w * 6144);
#pragma unroll
  for (int j = 0; j < 6; ++j)
    asm volatile("s_mov_b32 m0, %0\n\ts_nop 0\n\tbuffer_load_dwordx4 %1, %2, %3 offen lds" :: "s"(lb + j * 1024u), "v"(vo[j]), "s"(rs), "s"(soff) : "memory");
}

struct DmaJob { __amdgpu_buffer_rsrc_t rs; unsigned soff, lb; unsigned vo[6]; int valid; };
DI DmaJob mkjob(__amdgpu_buffer_rsrc_t rs, const unsigned (&vo)[6], unsigned soff, char* smem, int slot, int w) {
  DmaJob j; j.rs = rs; j.soff = soff; j.lb = (unsigned)(size_t)smem + (unsigned)(slot * SLOT + w * 6144); j.valid = 1;
#pragma unroll
  for (int i = 0; i < 6; ++i) j.vo[i] = vo[i];
  return j;
}
DI void fixjob(DmaJob& j) { j.soff = __builtin_amdgcn_readfirstlane(j.soff); j.lb = __builtin_amdgcn_readfirstlane(j.lb); }
DI void dma_piece(const DmaJob& jb, int j) {
  asm volatile("s_mov_b32 m0, %0\n\ts_nop 0\n\tbuffer_load_dwordx4 %1, %2, %3 offen lds" :: "s"(jb.lb + j * 1024u), "v"(jb.vo[j]), "s"(jb.rs), "s"(jb.soff) : "memory");
}

__global__ __launch_bounds__(256, 1) void k1_attn(const u16* __restrict__ xA, const u16* __restrict__ kA, const u16* __restrict__ vA, const u16* __restrict__ wqA,
                                                  const u16* __restrict__ xB, const u16* __restrict__ kB, const u16* __restrict__ vB, const u16* __restrict__ wqB,
                                                  u16* __restrict__ Og, unsigned* __restrict__ cnt) {
  extern __shared__ __attribute__((aligned(16))) char smem[];
  const int tid = threadIdx.x, l = tid & 63;
  const int w = __builtin_amdgcn_readfirstlane(tid >> 6);
  const int r = l & 15, g = l >> 4, q4 = (l & 15) >> 2, p4 = l & 3;
  const int wg = blockIdx.x, xcd = wg & 7, idx = wg >> 3;
  const int head = xcd * 4 + (idx & 3), b = idx >> 2;

  if (blockIdx.x == 0 && tid < 64) cnt[tid * 32] = 0u;
  DmaOff D;
#pragma unroll
  for (int j = 0; j < 6; ++j) {
    const int p = (w * 6 + j) * 64 + l;
    const int row = p / 24, chp = p % 24;
    D.k[j] = row * 384 + ((chp & ~7) | ((chp & 7) ^ ((row >> 1) & 7))) * 16;
    D.v[j] = row * 384 + ((chp & ~7) | ((chp & 7) ^ (((row >> 1) & 3) << 1))) * 16;
    D.w[j] = row * 12288 + ((chp & ~7) | ((chp & 7) ^ ((((row >> 1) & 1) << 1) | (((row >> 3) & 1) << 2)))) * 16;
    const int rx = p >> 3, cx = p & 7;
    D.x[j] = rx * 1280 + (cx ^ ((rx >> 1) & 7)) * 16;
  }
  const int kb0 = r * 384 + ((g) ^ (r >> 1)) * 16;
  const int kb1 = r * 384 + ((4 + g) ^ (r >> 1)) * 16;
  int vbs[4], wbs[4];
  {
    const int fv = ((2 * g + (q4 >> 1)) & 3) << 1;
    const int fw = ((q4 >> 1) << 1) | ((g & 1) << 2);
#pragma unroll
    for (int c = 0; c < 4; ++c) {
      vbs[c] = (4 * g + q4) * 384 + (((2 * c) ^ fv) + (p4 >> 1)) * 16 + 8 * (p4 & 1);
      wbs[c] = (8 * g + q4) * 384 + (((2 * c) ^ fw) + (p4 >> 1)) * 16 + 8 * (p4 & 1);
    }
  }
  const int xb0 = (48 * w + r) * 128 + ((g) ^ (r >> 1)) * 16;
  const int xb1 = (48 * w + r) * 128 + ((4 + g) ^ (r >> 1)) * 16;

  const unsigned xoff = (unsigned)b * (SQ * DM * 2);
  const unsigned woff = (unsigned)head * (DH * 2);
  const unsigned kvoff = (unsigned)(b * NH + head) * (SK * DH * 2);

  __amdgpu_buffer_rsrc_t rX = __builtin_amdgcn_make_buffer_rsrc((void*)xA, 0, NB * SQ * DM * 2, 0x00020000);
  __amdgpu_buffer_rsrc_t rW = __builtin_amdgcn_make_buffer_rsrc((void*)wqA, 0, DM * HD * 2, 0x00020000);
  __amdgpu_buffer_rsrc_t rK = __builtin_amdgcn_make_buffer_rsrc((void*)kA, 0, NB * NH * SK * DH * 2, 0x00020000);
  __amdgpu_buffer_rsrc_t rV = __builtin_amdgcn_make_buffer_rsrc((void*)vA, 0, NB * NH * SK * DH * 2, 0x00020000);
  const __amdgpu_buffer_rsrc_t rXn = __builtin_amdgcn_make_buffer_rsrc((void*)xB, 0, NB * SQ * DM * 2, 0x00020000);
  const __amdgpu_buffer_rsrc_t rWn = __builtin_amdgcn_make_buffer_rsrc((void*)wqB, 0, DM * HD * 2, 0x00020000);
  const __amdgpu_buffer_rsrc_t rKn = __builtin_amdgcn_make_buffer_rsrc((void*)kB, 0, NB * NH * SK * DH * 2, 0x00020000);
  const __amdgpu_buffer_rsrc_t rVn = __builtin_amdgcn_make_buffer_rsrc((void*)vB, 0, NB * NH * SK * DH * 2, 0x00020000);

#define ISSUE_X(R, i, slot) dma_tile(R, D.x, xoff + (i) * 128u, smem, slot, w)
#define ISSUE_W(R, i, slot) dma_tile(R, D.w, woff + (i) * (64u * 12288u), smem, slot, w)
#define ISSUE_K(t, slot) dma_tile(rK, D.k, kvoff + (t) * 24576u, smem, slot, w)
#define ISSUE_V(t, slot) dma_tile(rV, D.v, kvoff + (t) * 24576u, smem, slot, w)

#define JOB_X(R, i, slot) mkjob(R, D.x, xoff + (i) * 128u, smem, slot, w)
#define JOB_W(R, i, slot) mkjob(R, D.w, woff + (i) * (64u * 12288u), smem, slot, w)
#define JOB_K(t, slot) mkjob(rK, D.k, kvoff + (t) * 24576u, smem, slot, w)
#define JOB_V(t, slot) mkjob(rV, D.v, kvoff + (t) * 24576u, smem, slot, w)
  DmaJob DUMMY = JOB_X(rX, 0, 0); DUMMY.lb = (unsigned)(size_t)smem + 6u * SLOT + (unsigned)w * 1024u;

  ISSUE_X(rX, 0, 0); ISSUE_W(rW, 0, 1); ISSUE_X(rX, 1, 2); ISSUE_W(rW, 1, 3); ISSUE_X(rX, 2, 4); ISSUE_W(rW, 2, 5);

  const float QSCALE = 0.07216878364870322f * 1.4426950408889634f;
  constexpr std::true_type T_{}; constexpr std::false_type F_{};

  for (int unit = 0; unit < 2; ++unit) {
    f32x4 acc[12][3];
#pragma unroll
    for (int c = 0; c < 12; ++c)
#pragma unroll
      for (int qt = 0; qt < 3; ++qt) { acc[c][qt] = f32x4{0.f, 0.f, 0.f, 0.f}; asm volatile("" : "+a"(acc[c][qt])); }

    {
      bf16x8 xf[2][3];
      s16x4 wlo[4], whi[4];
      auto waddr = [&](const char* pw, int i) { const int ks1 = i / 12, c1 = i % 12; return pw + wbs[c1 & 3] + (32 * ks1) * 384 + (c1 >> 2) * 128; };
      auto qphase = [&](int sx, int sxn, const DmaJob& j0, const DmaJob& j1, auto hasNext_, int syncn) {
        constexpr bool hasNext = decltype(hasNext_)::value;
        const char* px = smem + sx * SLOT;
        const char* pw = px + SLOT;
        const char* pxn = smem + sxn * SLOT;
        const char* pwn = pxn + SLOT;
#pragma unroll
        for (int i = 0; i < 24; ++i) {
          const int ks = i / 12, c = i % 12;
          const bf16x8 wf = cat8(wlo[i & 3], whi[i & 3]);
          if constexpr (hasNext) {
            if (i == 21) { if (syncn == 12) { PHASE_SYNC(12); } else if (syncn == 18) { PHASE_SYNC(18); } else { PHASE_SYNC(24); } }
          }
          const bool ld = (i + 3 < 24) || hasNext;
          const char* an = (i + 3 < 24) ? waddr(pw, i + 3) : waddr(pwn, i + 3 - 24);
          mfma_a(acc[c][0], wf, xf[ks][0]);
          if (ld) wlo[(i + 3) & 3] = lds_tr(an);
          SB();
          mfma_a(acc[c][1], wf, xf[ks][1]);
          if (ld) whi[(i + 3) & 3] = lds_tr(an + 4 * 384);
          SB();
          mfma_a(acc[c][2], wf, xf[ks][2]);
          if (i < 3) xf[1][i] = lds_b128(px + xb1 + i * 2048);
          else if (i >= 21) { if constexpr (hasNext) xf[0][i - 21] = lds_b128(pxn + xb0 + (i - 21) * 2048); }
          else if (i < 15) { if (i & 1) dma_piece(j0, (i - 3) >> 1); }
          else dma_piece(j1, i - 15);
          SB();
        }
      };
      if (unit == 0) { PHASE_SYNC(24); } else { PHASE_SYNC(36); }
#pragma unroll
      for (int i = 0; i < 3; ++i) xf[0][i] = lds_b128(smem + xb0 + i * 2048);
#pragma unroll
      for (int i = 0; i < 3; ++i) { const char* a = waddr(smem + SLOT, i); wlo[i] = lds_tr(a); whi[i] = lds_tr(a + 4 * 384); }
      int sx = 0;
      for (int i = 0; i < 9; ++i) {
        const int sp = (sx == 0) ? 4 : sx - 2;
        const int sxn = (sx == 4) ? 0 : sx + 2;
        DmaJob j0, j1;
        if (i == 0) { j0 = JOB_W(rW, 2, 5); if (unit == 0) j0 = DUMMY; j1 = DUMMY; }
        else if (i < 8) { j0 = JOB_X(rX, i + 2, sp); j1 = JOB_W(rW, i + 2, sp + 1); }
        else { j0 = JOB_K(0, 2); j1 = JOB_K(1, 3); }
        fixjob(j0); fixjob(j1);
        qphase(sx, sxn, j0, j1, T_, i == 0 ? 24 : (i == 1 ? 18 : 12));
        sx = sxn;
      }
      qphase(0, 0, JOB_V(0, 4), JOB_K(2, 5), F_, 0);
    }

    MFMA_DRAIN();
    bf16x8 qf[3][6];
#pragma unroll
    for (int qt = 0; qt < 3; ++qt)
#pragma unroll
      for (int s = 0; s < 6; ++s) {
        asm volatile("" : "+a"(acc[2 * s][qt]), "+a"(acc[2 * s + 1][qt]));
        const f32x4 A = acc[2 * s][qt] * QSCALE, B = acc[2 * s + 1][qt] * QSCALE;
        u32x4 o = pair_fix(cvtpk(A[0], A[1]), cvtpk(A[2], A[3]), cvtpk(B[0], B[1]), cvtpk(B[2], B[3]));
        qf[qt][s] = __builtin_bit_cast(bf16x8, o);
        asm volatile("" : "+a"(qf[qt][s]));
        SB();
      }
#pragma unroll
    for (int c = 0; c < 12; ++c)
#pragma unroll
      for (int qt = 0; qt < 3; ++qt) { acc[c][qt] = f32x4{0.f, 0.f, 0.f, 0.f}; asm volatile("" : "+a"(acc[c][qt])); }

    float mref[3], lsum[3] = {0.f, 0.f, 0.f};
    f32x4 negm[3] = {f32x4{0.f, 0.f, 0.f, 0.f}, f32x4{0.f, 0.f, 0.f, 0.f}, f32x4{0.f, 0.f, 0.f, 0.f}};
    f32x4 Sc[2][3];
    bf16x8 Pp[3];
    bf16x8 kf[3];
    s16x4 vlo[3], vhi[3];

    auto sm = [&](const f32x4 (&S)[2][3], bf16x8 (&P)[3], float (&ps)[3]) {
#pragma unroll
      for (int qt = 0; qt < 3; ++qt) {
        float e[8];
#pragma unroll
        for (int i = 0; i < 8; ++i) e[i] = __builtin_amdgcn_exp2f(S[i >> 2][qt][i & 3]);
        ps[qt] = ((e[0] + e[1]) + (e[2] + e[3])) + ((e[4] + e[5]) + (e[6] + e[7]));
        u32x4 o = {cvtpk(e[0], e[1]), cvtpk(e[2], e[3]), cvtpk(e[4], e[5]), cvtpk(e[6], e[7])};
        P[qt] = __builtin_bit_cast(bf16x8, o);
      }
    };
    auto rowmax = [&](const f32x4 (&S)[2][3], int qt) {
      float mx = fmaxf(fmaxf(fmaxf(S[0][qt][0], S[0][qt][1]), fmaxf(S[0][qt][2], S[0][qt][3])),
                       fmaxf(fmaxf(S[1][qt][0], S[1][qt][1]), fmaxf(S[1][qt][2], S[1][qt][3])));
      mx = fmaxf(mx, __shfl_xor(mx, 16));
      mx = fmaxf(mx, __shfl_xor(mx, 32));
      return mx;
    };
    auto kaddr = [&](const char* pk, int i) { const int s1 = i >> 1, k21 = i & 1; return pk + ((s1 & 1) ? kb1 : kb0) + k21 * 6144 + (s1 >> 1) * 128; };
    auto vaddr = [&](const char* pv, int c1) { return pv + vbs[c1 & 3] + (c1 >> 2) * 128; };
    auto kbase = [&](int slot, int hb) { return (const char*)smem + slot * SLOT + hb * (2 * 6144); };
    auto vbase = [&](int slot, int ks) { return (const char*)smem + slot * SLOT + (32 * ks) * 384; };
    auto step = [&](auto doQK_, const char* pk, auto doPV_, const char* pvv, auto doSM_, const DmaJob& jb,
                    auto nQK_, const char* pkn, auto nPV_, const char* pvn, auto hasSync_, int syncn) {
      constexpr bool doQK = decltype(doQK_)::value, doPV = decltype(doPV_)::value, doSM = decltype(doSM_)::value;
      constexpr bool nQK = decltype(nQK_)::value, nPV = decltype(nPV_)::value, hasSync = decltype(hasSync_)::value;
      f32x4 Sn[2][3];
      u32x4 Pn[3];
      float ps[3] = {0.f, 0.f, 0.f};
      float ea = 0.f, eb = 0.f;
#pragma unroll
      for (int i = 0; i < 12; ++i) {
        const int s = i >> 1, k2 = i & 1, ri = i % 3, rn = (i + 2) % 3;
        if constexpr (hasSync) { if (i == 10) { if (syncn == 12) { PHASE_SYNC(12); } else { PHASE_SYNC(18); } } }
        const bool in = (i + 2 < 12);
        const bool ldK = in ? doQK : nQK, ldV = in ? doPV : nPV;
        const char* ka = in ? kaddr(pk, i + 2) : kaddr(pkn, i + 2 - 12);
        const char* va = in ? vaddr(pvv, i + 2) : vaddr(pvn, i + 2 - 12);
        const bf16x8 kc = kf[ri];
        const bf16x8 vc = cat8(vlo[ri], vhi[ri]);
        const int pq = (i - 1) >> 2, pj = (i - 1) & 3;
        const int eq = i >> 2, e0 = (i & 3) * 2;
        if constexpr (doQK) { if (s == 0) mfma_v0(Sn[k2][0], kc, qf[0][s], negm[0]); else mfma_v(Sn[k2][0], kc, qf[0][s]); }
        if (ldV) vlo[rn] = lds_tr(va);
        if constexpr (doSM) { if (i > 0) { unsigned pk2 = cvtpk(ea, eb); asm volatile("" : "+v"(pk2)); Pn[pq][pj] = pk2; } }
        SB();
        if constexpr (doQK) { if (s == 0) mfma_v0(Sn[k2][1], kc, qf[1][s], negm[1]); else mfma_v(Sn[k2][1], kc, qf[1][s]); }
        if (ldV) vhi[rn] = lds_tr(va + 16 * 384);
        if constexpr (doSM) { if (i > 0) { if (pj == 0) ps[pq] = fadd_s(ea, eb); else ps[pq] = fadd_s(ps[pq], ea); } }
        SB();
        if constexpr (doQK) { if (s == 0) mfma_v0(Sn[k2][2], kc, qf[2][s], negm[2]); else mfma_v(Sn[k2][2], kc, qf[2][s]); }
        if (ldK) kf[rn] = lds_b128(ka);
        if constexpr (doSM) { if (i > 0) { if (pj != 0) ps[pq] = fadd_s(ps[pq], eb); } }
        SB();
        if constexpr (doPV) mfma_a(acc[i][0], vc, Pp[0]);
        if constexpr (doSM) { ea = __builtin_amdgcn_exp2f(Sc[e0 >> 2][eq][e0 & 3]); asm volatile("" : "+v"(ea)); }
        SB();
        if constexpr (doPV) mfma_a(acc[i][1], vc, Pp[1]);
        if constexpr (doSM) { eb = __builtin_amdgcn_exp2f(Sc[e0 >> 2][eq][(e0 & 3) + 1]); asm volatile("" : "+v"(eb)); }
        SB();
        if constexpr (doPV) mfma_a(acc[i][2], vc, Pp[2]);
        if (i < 10) { if ((i & 1) == 0) dma_piece(jb, i >> 1); else if (i == 9) dma_piece(jb, 5); }
        if constexpr (doQK) { if (i == 2) asm volatile("" :: "v"(negm[0]), "v"(negm[1]), "v"(negm[2])); }
        SB();
      }
      if constexpr (doSM) {
        Pn[2][3] = cvtpk(ea, eb);
        ps[2] = fadd_s(ps[2], ea); ps[2] = fadd_s(ps[2], eb);
        const bool bad = !(ps[0] <= 1.0e12f) || !(ps[1] <= 1.0e12f) || !(ps[2] <= 1.0e12f);
        bf16x8 Pb[3];
#pragma unroll
        for (int qt = 0; qt < 3; ++qt) Pb[qt] = __builtin_bit_cast(bf16x8, Pn[qt]);
        if (__builtin_expect(__any(bad), 0)) {
          MFMA_DRAIN();
          if constexpr (doQK) asm volatile("" : "+v"(Sn[0][0]), "+v"(Sn[0][1]), "+v"(Sn[0][2]), "+v"(Sn[1][0]), "+v"(Sn[1][1]), "+v"(Sn[1][2]));
#pragma unroll
          for (int qt = 0; qt < 3; ++qt) {
            const float d = fmaxf(rowmax(Sc, qt), 0.f);
            const float f = __builtin_amdgcn_exp2f(-d);
            mref[qt] += d;
            negm[qt] = f32x4{-mref[qt], -mref[qt], -mref[qt], -mref[qt]};
            asm volatile("s_nop 1" : "+v"(negm[qt]));
            lsum[qt] *= f;
#pragma unroll
            for (int c = 0; c < 12; ++c) { asm volatile("" : "+a"(acc[c][qt])); acc[c][qt] *= f; asm volatile("" : "+a"(acc[c][qt])); SB(); }
#pragma unroll
            for (int k2 = 0; k2 < 2; ++k2) { Sc[k2][qt] -= d; if constexpr (doQK) Sn[k2][qt] -= d; }
          }
          sm(Sc, Pb, ps);
        }
#pragma unroll
        for (int qt = 0; qt < 3; ++qt) { lsum[qt] += ps[qt]; Pp[qt] = Pb[qt]; asm volatile("s_nop 0" : "+v"(Pp[qt])); }
      }
      if constexpr (doQK) {
#pragma unroll
        for (int qt = 0; qt < 3; ++qt) { Sc[0][qt] = Sn[0][qt]; Sc[1][qt] = Sn[1][qt]; }
      }
    };

    PHASE_SYNC(18);
    kf[0] = lds_b128(kaddr(kbase(2, 0), 0)); kf[1] = lds_b128(kaddr(kbase(2, 0), 1));
    step(T_, kbase(2, 0), F_, smem, F_, JOB_V(1, 0), T_, kbase(2, 1), F_, smem, F_, 0);
    asm volatile("s_nop 7\n\ts_nop 7" : "+v"(Sc[0][0]), "+v"(Sc[0][1]), "+v"(Sc[0][2]), "+v"(Sc[1][0]), "+v"(Sc[1][1]), "+v"(Sc[1][2]));
#pragma unroll
    for (int qt = 0; qt < 3; ++qt) {
      const float mx = rowmax(Sc, qt);
      mref[qt] = mx;
      negm[qt] = f32x4{-mx, -mx, -mx, -mx};
      asm volatile("s_nop 1" : "+v"(negm[qt]));
      Sc[0][qt] -= mx; Sc[1][qt] -= mx;
    }
    step(T_, kbase(2, 1), F_, smem, T_, JOB_K(3, 1), T_, kbase(3, 0), T_, vbase(4, 0), T_, 18);
    {
      int ksl = 3;
      for (int t = 1; t < 7; ++t) {
        const int vsl = (ksl == 5) ? 0 : ksl + 1;
        const int kfree = (ksl == 1) ? 5 : ksl - 2;
        const int vfree = (kfree == 5) ? 0 : kfree + 1;
        const int ksn = (ksl == 5) ? 1 : ksl + 2, vsn = (ksn == 5) ? 0 : ksn + 1;
        DmaJob j0, j1;
        if (t == 1) { j0 = JOB_V(2, 2); j1 = DUMMY; }
        else if (t < 6) { j0 = JOB_K(t + 2, kfree); j1 = JOB_V(t + 1, vfree); }
        else { j0 = JOB_V(7, 5); j1 = JOB_X(rXn, 0, 0); if (unit) j1 = DUMMY; }
        fixjob(j0); fixjob(j1);
        step(T_, kbase(ksl, 0), T_, vbase(vsl, 0), T_, j0, T_, kbase(ksl, 1), T_, vbase(vsl, 1), F_, 0);
        step(T_, kbase(ksl, 1), T_, vbase(vsl, 1), T_, j1, T_, kbase(ksn, 0), T_, vbase(vsn, 0), T_, t < 3 ? 18 : 12);
        ksl = ksn;
      }
    }
    {
      DmaJob j0 = JOB_W(rWn, 0, 1), j1 = JOB_X(rXn, 1, 2), j2 = JOB_W(rWn, 1, 3), j3 = JOB_X(rXn, 2, 4);
      if (unit) { j0 = DUMMY; j1 = DUMMY; j2 = DUMMY; j3 = DUMMY; }
      fixjob(j0); fixjob(j1); fixjob(j2); fixjob(j3);
      step(T_, kbase(3, 0), T_, vbase(4, 0), T_, j0, T_, kbase(3, 1), T_, vbase(4, 1), F_, 0);
      step(T_, kbase(3, 1), T_, vbase(4, 1), T_, j1, F_, smem, T_, vbase(5, 0), T_, 18);
      step(F_, smem, T_, vbase(5, 0), T_, j2, F_, smem, T_, vbase(5, 1), F_, 0);
      step(F_, smem, T_, vbase(5, 1), F_, j3, F_, smem, F_, smem, F_, 0);
    }

    MFMA_DRAIN();
    {
      u16* orow = Og + (size_t)unit * MROWS * HD + ((size_t)(3 * head) * MROWS + (b * SQ + 48 * w + r)) * 64 + 8 * g;
#pragma unroll
      for (int qt = 0; qt < 3; ++qt) {
        float lt = lsum[qt];
        lt += __shfl_xor(lt, 16);
        lt += __shfl_xor(lt, 32);
        const float inv = 1.0f / lt;
#pragma unroll
        for (int s = 0; s < 6; ++s) {
          asm volatile("" : "+a"(acc[2 * s][qt]), "+a"(acc[2 * s + 1][qt]));
          const f32x4 A = acc[2 * s][qt] * inv, B = acc[2 * s + 1][qt] * inv;
          u32x4 o = pair_fix(cvtpk(A[0], A[1]), cvtpk(A[2], A[3]), cvtpk(B[0], B[1]), cvtpk(B[2], B[3]));
          *reinterpret_cast<u32x4*>(orow + (size_t)(s >> 1) * MROWS * 64 + qt * 16 * 64 + 32 * (s & 1)) = o;
          SB();
        }
      }
    }
    STAMP();
    rX = rXn; rW = rWn; rK = rKn; rV = rVn;
  }
  asm volatile("s_waitcnt vmcnt(0)" ::: "memory");
}

constexpr int K2_STAGE = 24576 + 20480;
constexpr int K2_LDS = 3 * K2_STAGE;
constexpr size_t WS_O_BYTES = (size_t)2 * MROWS * HD * 2;
constexpr size_t WS_PART_OFF = WS_O_BYTES;
constexpr size_t WS_PART_BYTES = (size_t)64 * 4 * 8 * 15360;
constexpr size_t WS_CNT_OFF = WS_PART_OFF + WS_PART_BYTES;

__global__ __launch_bounds__(512, 2) void k2_oproj(const u16* __restrict__ Og, const u16* __restrict__ woA, const u16* __restrict__ woB,
                                                   u16* __restrict__ out, float* __restrict__ part, unsigned* __restrict__ cnt) {
  extern __shared__ __attribute__((aligned(16))) char smem[];
  const int tid = threadIdx.x, l = tid & 63;
  const int w = __builtin_amdgcn_readfirstlane(tid >> 6);
  const int r = l & 15, g = l >> 4, q4 = (l & 15) >> 2, p4 = l & 3;
  const int mw = w >> 1, nw = w & 1;
  const int wg = blockIdx.x, xcd = wg & 7, idx = wg >> 3;
  const int br = xcd >> 2, sk = xcd & 3, mt = idx >> 2, nt = idx & 3;
  const int tl = br * 32 + idx;
  const u16* wo = br ? woB : woA;
  const __amdgpu_buffer_rsrc_t rO = __builtin_amdgcn_make_buffer_rsrc((void*)(Og + (size_t)br * MROWS * HD), 0, MROWS * HD * 2, 0x00020000);
  const __amdgpu_buffer_rsrc_t rW = __builtin_amdgcn_make_buffer_rsrc((void*)wo, 0, HD * DM * 2, 0x00020000);
  unsigned vo[3], vw[3], lo_[3], lw_[3];
#pragma unroll
  for (int j = 0; j < 3; ++j) {
    const int pi = w + 8 * j;
    const int p = pi * 64 + l, rx = p >> 3, cx = p & 7;
    vo[j] = rx * 128 + (cx ^ ((rx >> 1) & 7)) * 16;
    lo_[j] = pi * 1024;
    int wi = w + 8 * j; if (wi >= 20) wi -= 4;
    const int pw = wi * 64 + l, row = pw / 20, ch = pw % 20;
    vw[j] = row * (DM * 2) + (ch ^ (((row >> 3) & 1) << 1)) * 16;
    lw_[j] = 24576 + wi * 1024;
  }
  const unsigned ooff = ((unsigned)(sk * 24) * MROWS + (unsigned)(mt * 192)) * 128u;
  const unsigned woff = (unsigned)(sk * 1536) * (DM * 2) + (unsigned)nt * 320;
  const unsigned lbase = (unsigned)(size_t)smem;
  auto issue1 = [&](int st, int buf, int j) {
    const unsigned lb = lbase + buf * K2_STAGE;
    if (j & 1) asm volatile("s_mov_b32 m0, %0\n\ts_nop 0\n\tbuffer_load_dwordx4 %1, %2, %3 offen lds" :: "s"(lb + lw_[j >> 1]), "v"(vw[j >> 1]), "s"(rW), "s"(woff + st * (64u * DM * 2)) : "memory");
    else       asm volatile("s_mov_b32 m0, %0\n\ts_nop 0\n\tbuffer_load_dwordx4 %1, %2, %3 offen lds" :: "s"(lb + lo_[j >> 1]), "v"(vo[j >> 1]), "s"(rO), "s"(ooff + st * (MROWS * 128u)) : "memory");
  };
  const int ob0 = (48 * mw + r) * 128 + ((g) ^ (r >> 1)) * 16;
  const int ob1 = (48 * mw + r) * 128 + ((4 + g) ^ (r >> 1)) * 16;
  int wb[5];
#pragma unroll
  for (int c = 0; c < 5; ++c) wb[c] = 24576 + (8 * g + q4) * 320 + (p4 >> 1) * 16 + 8 * (p4 & 1) + (((5 * nw + c) ^ (g & 1)) * 32);
#define ST2() do {} while (0)
  f32x4 acc[5][3] = {};
  bf16x8 of0[3], wf0[5], of1[3], wf1[5];
  auto ldfr = [&](bf16x8 (&of)[3], bf16x8 (&wf)[5], const char* pb, int ks) {
#pragma unroll
    for (int m = 0; m < 3; ++m) of[m] = lds_b128(pb + (ks ? ob1 : ob0) + m * 2048);
#pragma unroll
    for (int c = 0; c < 5; ++c) { const char* a0 = pb + wb[c] + (32 * ks) * 320; wf[c] = cat8(lds_tr(a0), lds_tr(a0 + 4 * 320)); }
  };
  ST2();
#pragma unroll
  for (int j = 0; j < 6; ++j) issue1(0, 0, j);
#pragma unroll
  for (int j = 0; j < 6; ++j) issue1(1, 1, j);
  asm volatile("s_waitcnt vmcnt(6)" ::: "memory");
  __builtin_amdgcn_s_barrier();
  asm volatile("" ::: "memory");
  ldfr(of0, wf0, smem, 0);
  int buf = 0;
  for (int st = 0; st < 24; ++st) {
    const char* pb = smem + buf * K2_STAGE;
    const int bn = (buf == 2) ? 0 : buf + 1, bp = (buf == 0) ? 2 : buf - 1;
    ST2();
    ldfr(of1, wf1, pb, 1);
    SB();
#pragma unroll
    for (int c = 0; c < 5; ++c) {
#pragma unroll
      for (int m = 0; m < 3; ++m) acc[c][m] = mfma16(wf0[c], of0[m], acc[c][m]);
      if (st + 2 < 24) { issue1(st + 2, bp, c); if (c == 4) issue1(st + 2, bp, 5); }
      SB();
    }
    ST2();
    if (st < 22) { asm volatile("s_waitcnt vmcnt(6) lgkmcnt(0)" ::: "memory"); } else { asm volatile("s_waitcnt vmcnt(0) lgkmcnt(0)" ::: "memory"); }
    __builtin_amdgcn_s_barrier();
    asm volatile("" ::: "memory");
    ST2();
    if (st + 1 < 24) ldfr(of0, wf0, smem + bn * K2_STAGE, 0);
    SB();
#pragma unroll
    for (int c = 0; c < 5; ++c)
#pragma unroll
      for (int m = 0; m < 3; ++m) acc[c][m] = mfma16(wf1[c], of1[m], acc[c][m]);
    buf = bn;
  }
  ST2();
  float* slab = part + ((size_t)(tl * 4) * 8) * 3840;
  const __amdgpu_buffer_rsrc_t rP = __builtin_amdgcn_make_buffer_rsrc((void*)slab, 0, 4 * 8 * 15360, 0x00020000);
#pragma unroll
  for (int c = 0; c < 5; ++c)
#pragma unroll
    for (int m = 0; m < 3; ++m) {
      const int owner = (c == 4) ? 3 : m;
      if (owner != sk) __builtin_amdgcn_raw_buffer_store_b128(__builtin_bit_cast(u32x4, acc[c][m]), rP, l * 16, (unsigned)((sk * 8 + w) * 15360 + (c * 3 + m) * 1024), 16);
    }
  asm volatile("s_waitcnt vmcnt(0)" ::: "memory");
  __syncthreads();
  if (tid == 0) __hip_atomic_fetch_add(cnt + tl * 32, 1u, __ATOMIC_RELAXED, __HIP_MEMORY_SCOPE_AGENT);
  ST2();
  if (w == 0) {
    unsigned spins = 0;
    while (__hip_atomic_load(cnt + tl * 32, __ATOMIC_RELAXED, __HIP_MEMORY_SCOPE_AGENT) < 4u) { __builtin_amdgcn_s_sleep(1); if (++spins > (1u << 24)) break; }
  }
  __syncthreads();
  ST2();
  u16* orow = out + (size_t)(mt * 192 + 48 * mw + r) * (2 * DM) + br * DM + nt * 160 + 80 * nw;
  auto fin = [&](auto J_) {
    constexpr int J = decltype(J_)::value;
    constexpr int NT = (J == 3) ? 3 : 4;
    u32x4 v[3][NT];
#pragma unroll
    for (int s2 = 0; s2 < 3; ++s2) {
      const int src = s2 + (s2 >= J ? 1 : 0);
#pragma unroll
      for (int i = 0; i < NT; ++i) {
        const int ti = (J == 3) ? (12 + i) : (i * 3 + J);
        v[s2][i] = __builtin_amdgcn_raw_buffer_load_b128(rP, l * 16, (unsigned)((src * 8 + w) * 15360 + ti * 1024), 16);
      }
    }
    f32x4 sum[NT];
#pragma unroll
    for (int i = 0; i < NT; ++i) {
      const f32x4 own = (J == 3) ? acc[4][i] : acc[i][J];
      f32x4 t[4];
#pragma unroll
      for (int s4 = 0; s4 < 4; ++s4) t[s4] = (s4 == J) ? own : __builtin_bit_cast(f32x4, v[s4 - (s4 > J ? 1 : 0)][i]);
      sum[i] = (t[0] + t[1]) + (t[2] + t[3]);
    }
    if constexpr (J == 3) {
#pragma unroll
      for (int m = 0; m < 3; ++m) {
        uint2 e2 = make_uint2(cvtpk(sum[m][0], sum[m][1]), cvtpk(sum[m][2], sum[m][3]));
        *reinterpret_cast<uint2*>(orow + (size_t)m * 16 * (2 * DM) + 64 + 4 * g) = e2;
      }
    } else {
#pragma unroll
      for (int c = 0; c < 4; c += 2) {
        const f32x4 A = sum[c], B = sum[c + 1];
        u32x4 o = pair_fix(cvtpk(A[0], A[1]), cvtpk(A[2], A[3]), cvtpk(B[0], B[1]), cvtpk(B[2], B[3]));
        *reinterpret_cast<u32x4*>(orow + (size_t)J * 16 * (2 * DM) + 16 * c + 8 * g) = o;
      }
    }
  };
  if (sk == 0) fin(std::integral_constant<int, 0>{});
  else if (sk == 1) fin(std::integral_constant<int, 1>{});
  else if (sk == 2) fin(std::integral_constant<int, 2>{});
  else fin(std::integral_constant<int, 3>{});
  ST2();
}

extern "C" void kernel_launch(void* const* d_in, const int* in_sizes, int n_in,
                              void* d_out, int out_size, void* d_ws, size_t ws_size,
                              hipStream_t stream) {
  const u16* xA = (const u16*)d_in[0]; const u16* kA = (const u16*)d_in[1]; const u16* vA = (const u16*)d_in[2];
  const u16* wqA = (const u16*)d_in[3]; const u16* woA = (const u16*)d_in[4];
  const u16* xB = (const u16*)d_in[5]; const u16* kB = (const u16*)d_in[6]; const u16* vB = (const u16*)d_in[7];
  const u16* wqB = (const u16*)d_in[8]; const u16* woB = (const u16*)d_in[9];
  u16* Og = (u16*)d_ws;
  float* part = (float*)((char*)d_ws + WS_PART_OFF);
  unsigned* cnt = (unsigned*)((char*)d_ws + WS_CNT_OFF);
  (void)hipFuncSetAttribute((const void*)k1_attn, hipFuncAttributeMaxDynamicSharedMemorySize, K1_LDS);
  (void)hipFuncSetAttribute((const void*)k2_oproj, hipFuncAttributeMaxDynamicSharedMemorySize, K2_LDS);
  hipLaunchKernelGGL(k1_attn, dim3(256), dim3(256), K1_LDS, stream, xA, kA, vA, wqA, xB, kB, vB, wqB, Og, cnt);
  hipLaunchKernelGGL(k2_oproj, dim3(256), dim3(512), K2_LDS, stream, Og, woA, woB, (u16*)d_out, part, cnt);
}
```

```cpp
#include <hip/hip_runtime.h>
#include <hip/hip_bf16.h>
#include <stdint.h>
#include <type_traits>

#define DI __device__ __forceinline__
typedef unsigned short u16;
typedef __attribute__((ext_vector_type(8))) __bf16 bf16x8;
typedef __attribute__((ext_vector_type(2))) __bf16 bf16x2;
typedef __attribute__((ext_vector_type(4))) short s16x4;
typedef __attribute__((ext_vector_type(8))) short s16x8;
typedef __attribute__((ext_vector_type(4))) float f32x4;
typedef __attribute__((ext_vector_type(2))) float f32x2;
typedef __attribute__((ext_vector_type(4))) unsigned u32x4;
typedef __attribute__((address_space(3))) void lds_void;
typedef __attribute__((address_space(3))) s16x4 lds_s16x4;

constexpr int NB = 8, SQ = 192, DM = 640, NH = 32, SK = 512, DH = 192;
constexpr int HD = NH * DH;
constexpr int MROWS = NB * SQ;
constexpr int SLOT = 24576;
constexpr int K1_LDS = 160 * 1024;

DI unsigned cvtpk(float lo, float hi) {
  f32x2 v = {lo, hi};
  bf16x2 b = __builtin_convertvector(v, bf16x2);
  return __builtin_bit_cast(unsigned, b);
}
DI f32x4 mfma16(bf16x8 a, bf16x8 b, f32x4 c) { return __builtin_amdgcn_mfma_f32_16x16x32_bf16(a, b, c, 0, 0, 0); }
DI void mfma_a(f32x4& c, bf16x8 a, bf16x8 b) { asm volatile("v_mfma_f32_16x16x32_bf16 %0, %1, %2, %0" : "+a"(c) : "v"(a), "v"(b)); }
DI void mfma_v(f32x4& c, bf16x8 a, bf16x8 b) { asm volatile("v_mfma_f32_16x16x32_bf16 %0, %1, %2, %0" : "+v"(c) : "v"(a), "a"(b)); }
DI void mfma_vv(f32x4& c, bf16x8 a, bf16x8 b) { asm volatile("v_mfma_f32_16x16x32_bf16 %0, %1, %2, %0" : "+v"(c) : "v"(a), "v"(b)); }
DI void mfma_v0(f32x4& d, bf16x8 a, bf16x8 b, f32x4 c) { asm volatile("v_mfma_f32_16x16x32_bf16 %0, %1, %2, %3" : "=&v"(d) : "v"(a), "a"(b), "v"(c)); }
DI float fadd_s(float a, float b) { float r = a + b; asm("" : "+v"(r)); return r; }
#define MFMA_DRAIN() asm volatile("s_nop 7\n\ts_nop 7" ::: "memory")
DI bf16x8 lds_b128(const char* p) { return *reinterpret_cast<const bf16x8*>(p); }
DI bf16x8 lds_tr2(const char* p0, const char* p1) {
  s16x4 lo = __builtin_amdgcn_ds_read_tr16_b64_v4i16((lds_s16x4*)p0);
  s16x4 hi = __builtin_amdgcn_ds_read_tr16_b64_v4i16((lds_s16x4*)p1);
  s16x8 v = __builtin_shufflevector(lo, hi, 0, 1, 2, 3, 4, 5, 6, 7);
  return __builtin_bit_cast(bf16x8, v);
}
DI s16x4 lds_tr(const char* p) { return __builtin_amdgcn_ds_read_tr16_b64_v4i16((lds_s16x4*)p); }
DI bf16x8 cat8(s16x4 lo, s16x4 hi) { s16x8 v = __builtin_shufflevector(lo, hi, 0, 1, 2, 3, 4, 5, 6, 7); return __builtin_bit_cast(bf16x8, v); }
#define SB() __builtin_amdgcn_sched_barrier(0)
#define WAITV(n) asm volatile("s_waitcnt vmcnt(" #n ")" ::: "memory")
#define STAMP() do {} while (0)
#define PHASE_SYNC(n) do { STAMP(); asm volatile("s_waitcnt vmcnt(" #n ") lgkmcnt(0)" ::: "memory"); STAMP(); __builtin_amdgcn_s_barrier(); asm volatile("" ::: "memory"); STAMP(); } while (0)

DI u32x4 pair_fix(unsigned a0, unsigned a1, unsigned b0, unsigned b1) {
  auto r0 = __builtin_amdgcn_permlane32_swap(a0, b0, false, false);
  auto r1 = __builtin_amdgcn_permlane32_swap(a1, b1, false, false);
  auto s0 = __builtin_amdgcn_permlane16_swap(r0[0], r0[1], false, false);
  auto s1 = __builtin_amdgcn_permlane16_swap(r1[0], r1[1], false, false);
  u32x4 o = {s0[0], s1[0], s0[1], s1[1]};
  return o;
}

constexpr int NP = 8;
struct DmaOff { unsigned k[NP], v[NP], w[NP], x[NP]; };

struct DmaJob { __amdgpu_buffer_rsrc_t rs; unsigned soff, lb; unsigned vo[NP]; };
DI DmaJob mkjob(__amdgpu_buffer_rsrc_t rs, const unsigned (&vo)[NP], unsigned soff, char* smem, int slot, int w) {
  DmaJob j; j.rs = rs; j.soff = soff; j.lb = (unsigned)(size_t)smem + (unsigned)(slot * SLOT + w * (NP * 1024));
#pragma unroll
  for (int i = 0; i < NP; ++i) j.vo[i] = vo[i];
  return j;
}
DI void fixjob(DmaJob& j) { j.soff = __builtin_amdgcn_readfirstlane(j.soff); j.lb = __builtin_amdgcn_readfirstlane(j.lb); }
DI void dma_piece(const DmaJob& jb, int j) {
  asm volatile("s_mov_b32 m0, %0\n\ts_nop 0\n\tbuffer_load_dwordx4 %1, %2, %3 offen lds" :: "s"(jb.lb + j * 1024u), "v"(jb.vo[j]), "s"(jb.rs), "s"(jb.soff) : "memory");
}

__global__ __launch_bounds__(256, 1) void k1_attn(const u16* __restrict__ xA, const u16* __restrict__ kA, const u16* __restrict__ vA, const u16* __restrict__ wqA,
                                                  const u16* __restrict__ xB, const u16* __restrict__ kB, const u16* __restrict__ vB, const u16* __restrict__ wqB,
                                                  u16* __restrict__ Og, unsigned* __restrict__ cnt) {
  extern __shared__ __attribute__((aligned(16))) char smem[];
  const int tid = threadIdx.x, l = tid & 63;
  const int w = __builtin_amdgcn_readfirstlane(tid >> 6);
  const int r = l & 15, g = l >> 4, q4 = (l & 15) >> 2, p4 = l & 3;
  const int wg = blockIdx.x, xcd = wg & 7, idx = wg >> 3;
  const int br = xcd >> 2, hp = (xcd & 3) * 4 + (idx & 3), b = idx >> 2;

  if (blockIdx.x == 0 && tid < 64) cnt[tid * 32] = 0u;
  DmaOff D;
#pragma unroll
  for (int j = 0; j < NP; ++j) {
    const int p = ((w < 3 ? w : 0) * NP + j) * 64 + l;
    const int row = p / 24, chp = p % 24;
    D.k[j] = row * 384 + ((chp & ~7) | ((chp & 7) ^ ((row >> 1) & 7))) * 16;
    D.v[j] = row * 384 + ((chp & ~7) | ((chp & 7) ^ (((row >> 1) & 3) << 1))) * 16;
    D.w[j] = row * 12288 + ((chp & ~7) | ((chp & 7) ^ ((((row >> 1) & 1) << 1) | (((row >> 3) & 1) << 2)))) * 16;
    const int rx = p >> 3, cx = p & 7;
    D.x[j] = rx * 1280 + (cx ^ ((rx >> 1) & 7)) * 16;
  }
  const int kb0 = r * 384 + ((g) ^ (r >> 1)) * 16;
  const int kb1 = r * 384 + ((4 + g) ^ (r >> 1)) * 16;
  int vbs[4], wbs[4];
  {
    const int fv = ((2 * g + (q4 >> 1)) & 3) << 1;
    const int fw = ((q4 >> 1) << 1) | ((g & 1) << 2);
#pragma unroll
    for (int c = 0; c < 4; ++c) {
      vbs[c] = (4 * g + q4) * 384 + (((2 * c) ^ fv) + (p4 >> 1)) * 16 + 8 * (p4 & 1);
      wbs[c] = (8 * g + q4) * 384 + (((2 * c) ^ fw) + (p4 >> 1)) * 16 + 8 * (p4 & 1);
    }
  }
  const int xb0 = (48 * w + r) * 128 + ((g) ^ (r >> 1)) * 16;
  const int xb1 = (48 * w + r) * 128 + ((4 + g) ^ (r >> 1)) * 16;

  const unsigned xoff = (unsigned)b * (SQ * DM * 2);
  const unsigned woff = (unsigned)hp * (2 * DH * 2);
  const unsigned kvoff = (unsigned)(b * NH + 2 * hp) * (SK * DH * 2);

  const __amdgpu_buffer_rsrc_t rX = __builtin_amdgcn_make_buffer_rsrc((void*)(br ? xB : xA), 0, NB * SQ * DM * 2, 0x00020000);
  const __amdgpu_buffer_rsrc_t rW = __builtin_amdgcn_make_buffer_rsrc((void*)(br ? wqB : wqA), 0, DM * HD * 2, 0x00020000);
  const __amdgpu_buffer_rsrc_t rK = __builtin_amdgcn_make_buffer_rsrc((void*)(br ? kB : kA), 0, NB * NH * SK * DH * 2, 0x00020000);
  const __amdgpu_buffer_rsrc_t rV = __builtin_amdgcn_make_buffer_rsrc((void*)(br ? vB : vA), 0, NB * NH * SK * DH * 2, 0x00020000);

#define JOB_X(i, slot) mkjob(rX, D.x, xoff + (i) * 128u, smem, slot, w)
#define JOB_W(hh, i, slot) mkjob(rW, D.w, woff + (hh) * 384u + (i) * (64u * 12288u), smem, slot, w)
#define JOB_K(t, slot) mkjob(rK, D.k, kvoff + (t) * 24576u, smem, slot, w)
#define JOB_V(t, slot) mkjob(rV, D.v, kvoff + (t) * 24576u, smem, slot, w)
#define ISSUE(job) do { const DmaJob j_ = (job); _Pragma("unroll") for (int q_ = 0; q_ < NP; ++q_) dma_piece(j_, q_); } while (0)
  const unsigned tvo = (unsigned)l * 128u, tlb = (unsigned)(size_t)smem + 6u * SLOT;
  int tm = 0, tq = 0;
  auto touch = [&]() {
    if (tm < 26) {
      const unsigned off = kvoff + (unsigned)(3 + (tm >> 1)) * 24576u + (unsigned)tq * 8192u;
      if (tm & 1) asm volatile("s_mov_b32 m0, %0\n\ts_nop 0\n\tbuffer_load_dword %1, %2, %3 offen lds" :: "s"(tlb), "v"(tvo), "s"(rV), "s"(off) : "memory");
      else        asm volatile("s_mov_b32 m0, %0\n\ts_nop 0\n\tbuffer_load_dword %1, %2, %3 offen lds" :: "s"(tlb), "v"(tvo), "s"(rK), "s"(off) : "memory");
      if (++tq == 3) { tq = 0; ++tm; }
    }
  };
  const bool loader = (w < 3);
#define PIECE(job, q, dotouch) do { if (loader) dma_piece(job, q); else if (dotouch) touch(); } while (0)
  DmaJob DUMMY = mkjob(rX, D.k, xoff, smem, 0, w);

  if (loader) { ISSUE(JOB_X(0, 0)); ISSUE(JOB_W(0, 0, 1)); ISSUE(JOB_W(1, 0, 2)); ISSUE(JOB_X(1, 3)); ISSUE(JOB_W(0, 1, 4)); ISSUE(JOB_W(1, 1, 5)); }

  const float QSCALE = 0.07216878364870322f * 1.4426950408889634f;
  constexpr std::true_type T_{}; constexpr std::false_type F_{};

  f32x4 acc[12][3];
  bf16x8 qf[3][6];
  bf16x8 q1[3][6];
  {
    f32x4 acv[12][3];
#pragma unroll
    for (int c = 0; c < 12; ++c)
#pragma unroll
      for (int qt = 0; qt < 3; ++qt) {
        acc[c][qt] = f32x4{0.f, 0.f, 0.f, 0.f}; asm volatile("" : "+a"(acc[c][qt]));
        acv[c][qt] = f32x4{0.f, 0.f, 0.f, 0.f};
        if (c < 9) asm volatile("" : "+a"(acv[c][qt])); else asm volatile("" : "+v"(acv[c][qt]));
      }
    bf16x8 xf[2][3];
    s16x4 wlo[4], whi[4];
    auto waddr = [&](const char* pw, int i) { const int ks1 = i / 12, c1 = i % 12; return pw + wbs[c1 & 3] + (32 * ks1) * 384 + (c1 >> 2) * 128; };
    auto qphase = [&](auto isB_, const char* px, const char* pw, const char* pxn, const char* pwn, const DmaJob& j0, const DmaJob& j1, auto hasNext_) {
      constexpr bool isB = decltype(isB_)::value, hasNext = decltype(hasNext_)::value;
#pragma unroll
      for (int i = 0; i < 24; ++i) {
        const int ks = i / 12, c = i % 12;
        const bf16x8 wf = cat8(wlo[i & 3], whi[i & 3]);
        if constexpr (hasNext) { if (i == 21) { PHASE_SYNC(24); } }
        const bool ld = (i + 3 < 24) || hasNext;
        const char* an = (i + 3 < 24) ? waddr(pw, i + 3) : waddr(pwn, i + 3 - 24);
        if constexpr (isB) mfma_a(acc[c][0], wf, xf[ks][0]); else { if (c < 9) mfma_a(acv[c][0], wf, xf[ks][0]); else mfma_vv(acv[c][0], wf, xf[ks][0]); }
        if (ld) wlo[(i + 3) & 3] = lds_tr(an);
        SB();
        if constexpr (isB) mfma_a(acc[c][1], wf, xf[ks][1]); else { if (c < 9) mfma_a(acv[c][1], wf, xf[ks][1]); else mfma_vv(acv[c][1], wf, xf[ks][1]); }
        if (ld) whi[(i + 3) & 3] = lds_tr(an + 4 * 384);
        SB();
        if constexpr (isB) mfma_a(acc[c][2], wf, xf[ks][2]); else { if (c < 9) mfma_a(acv[c][2], wf, xf[ks][2]); else mfma_vv(acv[c][2], wf, xf[ks][2]); }
        if constexpr (!isB) {
          if (i < 3) xf[1][i] = lds_b128(px + xb1 + i * 2048);
          else if (i < 19) { if (i & 1) PIECE(j0, (i - 3) >> 1, i == 3); }
        } else {
          if (i >= 21) { if constexpr (hasNext) xf[0][i - 21] = lds_b128(pxn + xb0 + (i - 21) * 2048); }
          else if (i < 20) { if (i % 5 != 4) { const int q = (i / 5) * 4 + (i % 5); if (q < 8) PIECE(j0, q, q == 0); else PIECE(j1, q - 8, q == 8); } }
        }
        SB();
      }
    };
    PHASE_SYNC(32);
#pragma unroll
    for (int i = 0; i < 3; ++i) xf[0][i] = lds_b128(smem + xb0 + i * 2048);
#pragma unroll
    for (int i = 0; i < 3; ++i) { const char* a = waddr(smem + SLOT, i); wlo[i] = lds_tr(a); whi[i] = lds_tr(a + 4 * 384); }
    for (int i = 0; i < 8; ++i) {
      const int s0 = (i & 1) * 3, s1 = 3 - s0;
      const char* px = smem + s0 * SLOT;
      const char* pxn = smem + s1 * SLOT;
      qphase(F_, px, px + SLOT, px, px + 2 * SLOT, JOB_W(1, i + 1, s1 + 2), JOB_W(1, i + 1, s1 + 2), T_);
      qphase(T_, px, px + 2 * SLOT, pxn, pxn + SLOT, JOB_X(i + 2, s0), JOB_W(0, i + 2, s0 + 1), T_);
    }
    qphase(F_, smem, smem + SLOT, smem, smem + 2 * SLOT, JOB_W(1, 9, 5), JOB_W(1, 9, 5), T_);
    qphase(T_, smem, smem + 2 * SLOT, smem + 3 * SLOT, smem + 4 * SLOT, JOB_K(0, 0), JOB_K(1, 1), T_);
    qphase(F_, smem + 3 * SLOT, smem + 4 * SLOT, smem, smem + 5 * SLOT, JOB_K(2, 2), JOB_K(2, 2), T_);
    qphase(T_, smem + 3 * SLOT, smem + 5 * SLOT, smem, smem, JOB_V(0, 3), JOB_V(1, 4), F_);

#pragma unroll
    for (int qt = 0; qt < 3; ++qt)
#pragma unroll
      for (int s = 0; s < 6; ++s) {
        if (2 * s < 9) asm volatile("" : "+a"(acv[2 * s][qt]));
        if (2 * s + 1 < 9) asm volatile("" : "+a"(acv[2 * s + 1][qt]));
        const f32x4 A = acv[2 * s][qt] * QSCALE, B = acv[2 * s + 1][qt] * QSCALE;
        u32x4 o = pair_fix(cvtpk(A[0], A[1]), cvtpk(A[2], A[3]), cvtpk(B[0], B[1]), cvtpk(B[2], B[3]));
        qf[qt][s] = __builtin_bit_cast(bf16x8, o);
        asm volatile("" : "+a"(qf[qt][s]));
        if (s % 3 == 2) SB();
      }
    MFMA_DRAIN();
#pragma unroll
    for (int qt = 0; qt < 3; ++qt)
#pragma unroll
      for (int s = 0; s < 6; ++s) {
        asm volatile("" : "+a"(acc[2 * s][qt]), "+a"(acc[2 * s + 1][qt]));
        const f32x4 A = acc[2 * s][qt] * QSCALE, B = acc[2 * s + 1][qt] * QSCALE;
        u32x4 o = pair_fix(cvtpk(A[0], A[1]), cvtpk(A[2], A[3]), cvtpk(B[0], B[1]), cvtpk(B[2], B[3]));
        q1[qt][s] = __builtin_bit_cast(bf16x8, o);
        asm volatile("" : "+v"(q1[qt][s]));
        if (s % 3 == 2) SB();
      }
  }

  int sp = 2, sc = 0, sn = 1;
  for (int unit = 0; unit < 2; ++unit) {
#pragma unroll
    for (int c = 0; c < 12; ++c)
#pragma unroll
      for (int qt = 0; qt < 3; ++qt) { acc[c][qt] = f32x4{0.f, 0.f, 0.f, 0.f}; asm volatile("" : "+a"(acc[c][qt])); }

    float mref[3], lsum[3] = {0.f, 0.f, 0.f};
    f32x4 negm[3] = {f32x4{0.f, 0.f, 0.f, 0.f}, f32x4{0.f, 0.f, 0.f, 0.f}, f32x4{0.f, 0.f, 0.f, 0.f}};
    f32x4 Sc[2][3];
    bf16x8 Pp[3];
    bf16x8 kf[3];
    s16x4 vlo[3], vhi[3];

    auto sm = [&](const f32x4 (&S)[2][3], bf16x8 (&P)[3], float (&ps)[3]) {
#pragma unroll
      for (int qt = 0; qt < 3; ++qt) {
        float e[8];
#pragma unroll
        for (int i = 0; i < 8; ++i) e[i] = __builtin_amdgcn_exp2f(S[i >> 2][qt][i & 3]);
        ps[qt] = ((e[0] + e[1]) + (e[2] + e[3])) + ((e[4] + e[5]) + (e[6] + e[7]));
        u32x4 o = {cvtpk(e[0], e[1]), cvtpk(e[2], e[3]), cvtpk(e[4], e[5]), cvtpk(e[6], e[7])};
        P[qt] = __builtin_bit_cast(bf16x8, o);
      }
    };
    auto rowmax = [&](const f32x4 (&S)[2][3], int qt) {
      float mx = fmaxf(fmaxf(fmaxf(S[0][qt][0], S[0][qt][1]), fmaxf(S[0][qt][2], S[0][qt][3])),
                       fmaxf(fmaxf(S[1][qt][0], S[1][qt][1]), fmaxf(S[1][qt][2], S[1][qt][3])));
      mx = fmaxf(mx, __shfl_xor(mx, 16));
      mx = fmaxf(mx, __shfl_xor(mx, 32));
      return mx;
    };
    auto kaddr = [&](const char* pk, int i) { const int s1 = i >> 1, k21 = i & 1; return pk + ((s1 & 1) ? kb1 : kb0) + k21 * 6144 + (s1 >> 1) * 128; };
    auto vaddr = [&](const char* pv, int c1) { return pv + vbs[c1 & 3] + (c1 >> 2) * 128; };
    auto kbase = [&](int slot, int hb) { return (const char*)smem + slot * SLOT + hb * (2 * 6144); };
    auto vbase = [&](int slot, int ks) { return (const char*)smem + slot * SLOT + (32 * ks) * 384; };
    auto step = [&](auto doQK_, const char* pk, auto doPV_, const char* pvv, auto doSM_, auto hasJob_, const DmaJob& jb,
                    auto nQK_, const char* pkn, auto nPV_, const char* pvn, auto hasSync_) {
      constexpr bool hasJob = decltype(hasJob_)::value;
      constexpr bool doQK = decltype(doQK_)::value, doPV = decltype(doPV_)::value, doSM = decltype(doSM_)::value;
      constexpr bool nQK = decltype(nQK_)::value, nPV = decltype(nPV_)::value, hasSync = decltype(hasSync_)::value;
      f32x4 Sn[2][3];
      u32x4 Pn[3];
      float ps[3] = {0.f, 0.f, 0.f};
      float ea = 0.f, eb = 0.f;
#pragma unroll
      for (int i = 0; i < 12; ++i) {
        const int s = i >> 1, k2 = i & 1, ri = i % 3, rn = (i + 2) % 3;
        if constexpr (hasSync) { if (i == 10) { PHASE_SYNC(16); } }
        const bool in = (i + 2 < 12);
        const bool ldK = in ? doQK : nQK, ldV = in ? doPV : nPV;
        const char* ka = in ? kaddr(pk, i + 2) : kaddr(pkn, i + 2 - 12);
        const char* va = in ? vaddr(pvv, i + 2) : vaddr(pvn, i + 2 - 12);
        const bf16x8 kc = kf[ri];
        const bf16x8 vc = cat8(vlo[ri], vhi[ri]);
        const int pq = (i - 1) >> 2, pj = (i - 1) & 3;
        const int eq = i >> 2, e0 = (i & 3) * 2;
        if constexpr (doQK) { if (s == 0) mfma_v0(Sn[k2][0], kc, qf[0][s], negm[0]); else mfma_v(Sn[k2][0], kc, qf[0][s]); }
        if (ldV) vlo[rn] = lds_tr(va);
        if constexpr (doSM) { if (i > 0) { unsigned pk2 = cvtpk(ea, eb); asm volatile("" : "+v"(pk2)); Pn[pq][pj] = pk2; } }
        SB();
        if constexpr (doQK) { if (s == 0) mfma_v0(Sn[k2][1], kc, qf[1][s], negm[1]); else mfma_v(Sn[k2][1], kc, qf[1][s]); }
        if (ldV) vhi[rn] = lds_tr(va + 16 * 384);
        if constexpr (doSM) { if (i > 0) { if (pj == 0) ps[pq] = fadd_s(ea, eb); else ps[pq] = fadd_s(ps[pq], ea); } }
        SB();
        if constexpr (doQK) { if (s == 0) mfma_v0(Sn[k2][2], kc, qf[2][s], negm[2]); else mfma_v(Sn[k2][2], kc, qf[2][s]); }
        if (ldK) kf[rn] = lds_b128(ka);
        if constexpr (doSM) { if (i > 0) { if (pj != 0) ps[pq] = fadd_s(ps[pq], eb); } }
        SB();
        if constexpr (doPV) mfma_a(acc[i][0], vc, Pp[0]);
        if constexpr (doSM) { ea = __builtin_amdgcn_exp2f(Sc[e0 >> 2][eq][e0 & 3]); asm volatile("" : "+v"(ea)); }
        SB();
        if constexpr (doPV) mfma_a(acc[i][1], vc, Pp[1]);
        if constexpr (doSM) { eb = __builtin_amdgcn_exp2f(Sc[e0 >> 2][eq][(e0 & 3) + 1]); asm volatile("" : "+v"(eb)); }
        SB();
        if constexpr (doPV) mfma_a(acc[i][2], vc, Pp[2]);
        if constexpr (hasJob) { if (i < 8) PIECE(jb, i, hasSync ? (i == 0) : (i == 0 || i == 4)); }
        if constexpr (doQK) { if (i == 2) asm volatile("" :: "v"(negm[0]), "v"(negm[1]), "v"(negm[2])); }
        SB();
      }
      if constexpr (doSM) {
        Pn[2][3] = cvtpk(ea, eb);
        ps[2] = fadd_s(ps[2], ea); ps[2] = fadd_s(ps[2], eb);
        const bool bad = !(ps[0] <= 1.0e12f) || !(ps[1] <= 1.0e12f) || !(ps[2] <= 1.0e12f);
        bf16x8 Pb[3];
#pragma unroll
        for (int qt = 0; qt < 3; ++qt) Pb[qt] = __builtin_bit_cast(bf16x8, Pn[qt]);
        if (__builtin_expect(__any(bad), 0)) {
          MFMA_DRAIN();
          if constexpr (doQK) asm volatile("" : "+v"(Sn[0][0]), "+v"(Sn[0][1]), "+v"(Sn[0][2]), "+v"(Sn[1][0]), "+v"(Sn[1][1]), "+v"(Sn[1][2]));
#pragma unroll
          for (int qt = 0; qt < 3; ++qt) {
            const float d = fmaxf(rowmax(Sc, qt), 0.f);
            const float f = __builtin_amdgcn_exp2f(-d);
            mref[qt] += d;
            negm[qt] = f32x4{-mref[qt], -mref[qt], -mref[qt], -mref[qt]};
            asm volatile("s_nop 1" : "+v"(negm[qt]));
            lsum[qt] *= f;
#pragma unroll
            for (int c = 0; c < 12; ++c) { asm volatile("" : "+a"(acc[c][qt])); acc[c][qt] *= f; asm volatile("" : "+a"(acc[c][qt])); SB(); }
#pragma unroll
            for (int k2 = 0; k2 < 2; ++k2) { Sc[k2][qt] -= d; if constexpr (doQK) Sn[k2][qt] -= d; }
          }
          sm(Sc, Pb, ps);
        }
#pragma unroll
        for (int qt = 0; qt < 3; ++qt) { lsum[qt] += ps[qt]; Pp[qt] = Pb[qt]; asm volatile("s_nop 0" : "+v"(Pp[qt])); }
      }
      if constexpr (doQK) {
#pragma unroll
        for (int qt = 0; qt < 3; ++qt) { Sc[0][qt] = Sn[0][qt]; Sc[1][qt] = Sn[1][qt]; }
      }
    };

    const int G0 = unit * 8;
    auto jobK = [&](int G, int slot) { return JOB_K(G < 15 ? G : 15, slot); };
    auto jobV = [&](int G, int slot) { return JOB_V(G < 15 ? G : 15, slot); };
    if (unit == 0) { PHASE_SYNC(32); } else { PHASE_SYNC(58); }
    {
      kf[0] = lds_b128(kaddr(kbase(sc, 0), 0)); kf[1] = lds_b128(kaddr(kbase(sc, 0), 1));
      step(T_, kbase(sc, 0), F_, smem, F_, T_, jobV(G0 + 2, 3 + sp), T_, kbase(sc, 1), F_, smem, F_);
      asm volatile("s_nop 7\n\ts_nop 7" : "+v"(Sc[0][0]), "+v"(Sc[0][1]), "+v"(Sc[0][2]), "+v"(Sc[1][0]), "+v"(Sc[1][1]), "+v"(Sc[1][2]));
#pragma unroll
      for (int qt = 0; qt < 3; ++qt) {
        const float mx = rowmax(Sc, qt);
        mref[qt] = mx;
        negm[qt] = f32x4{-mx, -mx, -mx, -mx};
        asm volatile("s_nop 1" : "+v"(negm[qt]));
        Sc[0][qt] -= mx; Sc[1][qt] -= mx;
      }
      step(T_, kbase(sc, 1), F_, smem, T_, F_, DUMMY, T_, kbase(sn, 0), T_, vbase(3 + sc, 0), T_);
      { const int x = sp; sp = sc; sc = sn; sn = x; }
      step(T_, kbase(sc, 0), T_, vbase(3 + sp, 0), T_, T_, jobK(G0 + 3, sp), T_, kbase(sc, 1), T_, vbase(3 + sp, 1), F_);
      step(T_, kbase(sc, 1), T_, vbase(3 + sp, 1), T_, F_, DUMMY, T_, kbase(sn, 0), T_, vbase(3 + sc, 0), T_);
      { const int x = sp; sp = sc; sc = sn; sn = x; }
    }
    for (int t = 2; t < 7; ++t) {
      step(T_, kbase(sc, 0), T_, vbase(3 + sp, 0), T_, T_, jobK(G0 + t + 2, sp), T_, kbase(sc, 1), T_, vbase(3 + sp, 1), F_);
      step(T_, kbase(sc, 1), T_, vbase(3 + sp, 1), T_, T_, jobV(G0 + t + 1, 3 + sn), T_, kbase(sn, 0), T_, vbase(3 + sc, 0), T_);
      { const int x = sp; sp = sc; sc = sn; sn = x; }
    }
    {
      step(T_, kbase(sc, 0), T_, vbase(3 + sp, 0), T_, T_, jobK(G0 + 9, sp), T_, kbase(sc, 1), T_, vbase(3 + sp, 1), F_);
      step(T_, kbase(sc, 1), T_, vbase(3 + sp, 1), T_, T_, jobV(G0 + 8, 3 + sn), F_, smem, T_, vbase(3 + sc, 0), T_);
      { const int x = sp; sp = sc; sc = sn; sn = x; }
      step(F_, smem, T_, vbase(3 + sp, 0), T_, T_, jobK(G0 + 10, sp), F_, smem, T_, vbase(3 + sp, 1), F_);
      step(F_, smem, T_, vbase(3 + sp, 1), F_, T_, jobV(G0 + 9, 3 + sn), F_, smem, F_, smem, F_);
    }

    MFMA_DRAIN();
    {
      u16* orow = Og + (size_t)br * MROWS * HD + ((size_t)(3 * (2 * hp + unit)) * MROWS + (b * SQ + 48 * w + r)) * 64 + 8 * g;
#pragma unroll
      for (int qt = 0; qt < 3; ++qt) {
        float lt = lsum[qt];
        lt += __shfl_xor(lt, 16);
        lt += __shfl_xor(lt, 32);
        const float inv = 1.0f / lt;
#pragma unroll
        for (int s = 0; s < 6; ++s) {
          asm volatile("" : "+a"(acc[2 * s][qt]), "+a"(acc[2 * s + 1][qt]));
          const f32x4 A = acc[2 * s][qt] * inv, B = acc[2 * s + 1][qt] * inv;
          u32x4 o = pair_fix(cvtpk(A[0], A[1]), cvtpk(A[2], A[3]), cvtpk(B[0], B[1]), cvtpk(B[2], B[3]));
          *reinterpret_cast<u32x4*>(orow + (size_t)(s >> 1) * MROWS * 64 + qt * 16 * 64 + 32 * (s & 1)) = o;
          if (s % 3 == 2) SB();
        }
      }
    }
    STAMP();
#pragma unroll
    for (int qt = 0; qt < 3; ++qt)
#pragma unroll
      for (int s = 0; s < 6; ++s) { qf[qt][s] = q1[qt][s]; asm volatile("" : "+a"(qf[qt][s])); }
  }
  asm volatile("s_waitcnt vmcnt(0)" ::: "memory");
}

constexpr int K2_STAGE = 24576 + 20480;
constexpr int K2_LDS = 3 * K2_STAGE;
#define K2_PF 5
constexpr size_t WS_O_BYTES = (size_t)2 * MROWS * HD * 2;
constexpr size_t WS_PART_OFF = WS_O_BYTES;
constexpr size_t WS_PART_BYTES = (size_t)64 * 4 * 8 * 15360;
constexpr size_t WS_CNT_OFF = WS_PART_OFF + WS_PART_BYTES;

__global__ __launch_bounds__(576, 2) void k2_oproj(const u16* __restrict__ Og, const u16* __restrict__ woA, const u16* __restrict__ woB,
                                                   u16* __restrict__ out, float* __restrict__ part, unsigned* __restrict__ cnt) {
  extern __shared__ __attribute__((aligned(16))) char smem[];
  const int tid = threadIdx.x, l = tid & 63;
  const int w = __builtin_amdgcn_readfirstlane(tid >> 6);
  const int r = l & 15, g = l >> 4, q4 = (l & 15) >> 2, p4 = l & 3;
  const int mw = w >> 1, nw = w & 1;
  const int wg = blockIdx.x, xcd = wg & 7, idx = wg >> 3;
  const int br = xcd >> 2, sk = xcd & 3, mt = idx >> 2, nt = idx & 3;
  const int tl = br * 32 + idx;
  const u16* wo = br ? woB : woA;
  const __amdgpu_buffer_rsrc_t rO = __builtin_amdgcn_make_buffer_rsrc((void*)(Og + (size_t)br * MROWS * HD), 0, MROWS * HD * 2, 0x00020000);
  const __amdgpu_buffer_rsrc_t rW = __builtin_amdgcn_make_buffer_rsrc((void*)wo, 0, HD * DM * 2, 0x00020000);
  const unsigned ooff = ((unsigned)(sk * 24) * MROWS + (unsigned)(mt * 192)) * 128u;
  const unsigned woff = (unsigned)(sk * 1536) * (DM * 2) + (unsigned)nt * 320;
  if (w == 8) {
    const unsigned to = (l < 48) ? (unsigned)(nt * 48 + l) * 128u : 0x7fffffffu;
    const unsigned tw = (l < 24) ? (unsigned)(mt * 8 + l / 3) * (DM * 2) + (unsigned)(l % 3) * 128u : 0x7fffffffu;
    unsigned junk = 0;
    auto tch = [&](int st) {
      junk ^= __builtin_amdgcn_raw_buffer_load_b32(rO, to, ooff + st * (MROWS * 128u), 0);
      junk ^= __builtin_amdgcn_raw_buffer_load_b32(rW, tw, (woff & ~127u) + st * (64u * DM * 2), 0);
    };
#pragma unroll
    for (int st = 2; st < 2 + K2_PF; ++st) tch(st);
    __builtin_amdgcn_s_barrier();
    for (int st = 0; st < 24; ++st) {
      if (st + 2 + K2_PF < 24) tch(st + 2 + K2_PF);
      __builtin_amdgcn_s_barrier();
    }
    if (junk == 0x9e3779b9u) cnt[tl * 32 + 1] = junk;
    return;
  }
  unsigned vo[3], vw[3], lo_[3], lw_[3];
#pragma unroll
  for (int j = 0; j < 3; ++j) {
    const int pi = w + 8 * j;
    const int p = pi * 64 + l, rx = p >> 3, cx = p & 7;
    vo[j] = rx * 128 + (cx ^ ((rx >> 1) & 7)) * 16;
    lo_[j] = pi * 1024;
    int wi = w + 8 * j; if (wi >= 20) wi -= 4;
    const int pw = wi * 64 + l, row = pw / 20, ch = pw % 20;
    vw[j] = row * (DM * 2) + (ch ^ (((row >> 3) & 1) << 1)) * 16;
    lw_[j] = 24576 + wi * 1024;
  }
  const unsigned lbase = (unsigned)(size_t)smem;
  auto issue1 = [&](int st, int buf, int j) {
    const unsigned lb = lbase + buf * K2_STAGE;
    if (j & 1) asm volatile("s_mov_b32 m0, %0\n\ts_nop 0\n\tbuffer_load_dwordx4 %1, %2, %3 offen lds" :: "s"(lb + lw_[j >> 1]), "v"(vw[j >> 1]), "s"(rW), "s"(woff + st * (64u * DM * 2)) : "memory");
    else       asm volatile("s_mov_b32 m0, %0\n\ts_nop 0\n\tbuffer_load_dwordx4 %1, %2, %3 offen lds" :: "s"(lb + lo_[j >> 1]), "v"(vo[j >> 1]), "s"(rO), "s"(ooff + st * (MROWS * 128u)) : "memory");
  };
  const int ob0 = (48 * mw + r) * 128 + ((g) ^ (r >> 1)) * 16;
  const int ob1 = (48 * mw + r) * 128 + ((4 + g) ^ (r >> 1)) * 16;
  int wb[5];
#pragma unroll
  for (int c = 0; c < 5; ++c) wb[c] = 24576 + (8 * g + q4) * 320 + (p4 >> 1) * 16 + 8 * (p4 & 1) + (((5 * nw + c) ^ (g & 1)) * 32);
#define ST2() do {} while (0)
  f32x4 acc[5][3] = {};
  bf16x8 of0[3], wf0[5], of1[3], wf1[5];
  auto ldfr = [&](bf16x8 (&of)[3], bf16x8 (&wf)[5], const char* pb, int ks) {
#pragma unroll
    for (int m = 0; m < 3; ++m) of[m] = lds_b128(pb + (ks ? ob1 : ob0) + m * 2048);
#pragma unroll
    for (int c = 0; c < 5; ++c) { const char* a0 = pb + wb[c] + (32 * ks) * 320; wf[c] = cat8(lds_tr(a0), lds_tr(a0 + 4 * 320)); }
  };
  ST2();
#pragma unroll
  for (int j = 0; j < 6; ++j) issue1(0, 0, j);
#pragma unroll
  for (int j = 0; j < 6; ++j) issue1(1, 1, j);
  asm volatile("s_waitcnt vmcnt(6)" ::: "memory");
  __builtin_amdgcn_s_barrier();
  asm volatile("" ::: "memory");
  ldfr(of0, wf0, smem, 0);
  int buf = 0;
  for (int st = 0; st < 24; ++st) {
    const char* pb = smem + buf * K2_STAGE;
    const int bn = (buf == 2) ? 0 : buf + 1, bp = (buf == 0) ? 2 : buf - 1;
    ST2();
    ldfr(of1, wf1, pb, 1);
    SB();
#pragma unroll
    for (int c = 0; c < 5; ++c) {
#pragma unroll
      for (int m = 0; m < 3; ++m) acc[c][m] = mfma16(wf0[c], of0[m], acc[c][m]);
      if (st + 2 < 24) { issue1(st + 2, bp, c); if (c == 4) issue1(st + 2, bp, 5); }
      SB();
    }
    ST2();
    if (st < 22) { asm volatile("s_waitcnt vmcnt(6) lgkmcnt(0)" ::: "memory"); } else { asm volatile("s_waitcnt vmcnt(0) lgkmcnt(0)" ::: "memory"); }
    __builtin_amdgcn_s_barrier();
    asm volatile("" ::: "memory");
    ST2();
    if (st + 1 < 24) ldfr(of0, wf0, smem + bn * K2_STAGE, 0);
    SB();
#pragma unroll
    for (int c = 0; c < 5; ++c)
#pragma unroll
      for (int m = 0; m < 3; ++m) acc[c][m] = mfma16(wf1[c], of1[m], acc[c][m]);
    buf = bn;
  }
  ST2();
  float* slab = part + ((size_t)(tl * 4) * 8) * 3840;
  const __amdgpu_buffer_rsrc_t rP = __builtin_amdgcn_make_buffer_rsrc((void*)slab, 0, 4 * 8 * 15360, 0x00020000);
#pragma unroll
  for (int c = 0; c < 5; ++c)
#pragma unroll
    for (int m = 0; m < 3; ++m) {
      const int owner = (c == 4) ? 3 : m;
      if (owner != sk) __builtin_amdgcn_raw_buffer_store_b128(__builtin_bit_cast(u32x4, acc[c][m]), rP, l * 16, (unsigned)((sk * 8 + w) * 15360 + (c * 3 + m) * 1024), 16);
    }
  asm volatile("s_waitcnt vmcnt(0)" ::: "memory");
  __syncthreads();
  if (tid == 0) __hip_atomic_fetch_add(cnt + tl * 32, 1u, __ATOMIC_RELAXED, __HIP_MEMORY_SCOPE_AGENT);
  ST2();
  if (w == 0) {
    unsigned spins = 0;
    while (__hip_atomic_load(cnt + tl * 32, __ATOMIC_RELAXED, __HIP_MEMORY_SCOPE_AGENT) < 4u) { __builtin_amdgcn_s_sleep(1); if (++spins > (1u << 24)) break; }
  }
  __syncthreads();
  ST2();
  u16* orow = out + (size_t)(mt * 192 + 48 * mw + r) * (2 * DM) + br * DM + nt * 160 + 80 * nw;
  auto fin = [&](auto J_) {
    constexpr int J = decltype(J_)::value;
    constexpr int NT = (J == 3) ? 3 : 4;
    u32x4 v[3][NT];
#pragma unroll
    for (int s2 = 0; s2 < 3; ++s2) {
      const int src = s2 + (s2 >= J ? 1 : 0);
#pragma unroll
      for (int i = 0; i < NT; ++i) {
        const int ti = (J == 3) ? (12 + i) : (i * 3 + J);
        v[s2][i] = __builtin_amdgcn_raw_buffer_load_b128(rP, l * 16, (unsigned)((src * 8 + w) * 15360 + ti * 1024), 16);
      }
    }
    f32x4 sum[NT];
#pragma unroll
    for (int i = 0; i < NT; ++i) {
      const f32x4 own = (J == 3) ? acc[4][i] : acc[i][J];
      f32x4 t[4];
#pragma unroll
      for (int s4 = 0; s4 < 4; ++s4) t[s4] = (s4 == J) ? own : __builtin_bit_cast(f32x4, v[s4 - (s4 > J ? 1 : 0)][i]);
      sum[i] = (t[0] + t[1]) + (t[2] + t[3]);
    }
    if constexpr (J == 3) {
#pragma unroll
      for (int m = 0; m < 3; ++m) {
        uint2 e2 = make_uint2(cvtpk(sum[m][0], sum[m][1]), cvtpk(sum[m][2], sum[m][3]));
        *reinterpret_cast<uint2*>(orow + (size_t)m * 16 * (2 * DM) + 64 + 4 * g) = e2;
      }
    } else {
#pragma unroll
      for (int c = 0; c < 4; c += 2) {
        const f32x4 A = sum[c], B = sum[c + 1];
        u32x4 o = pair_fix(cvtpk(A[0], A[1]), cvtpk(A[2], A[3]), cvtpk(B[0], B[1]), cvtpk(B[2], B[3]));
        *reinterpret_cast<u32x4*>(orow + (size_t)J * 16 * (2 * DM) + 16 * c + 8 * g) = o;
      }
    }
  };
  if (sk == 0) fin(std::integral_constant<int, 0>{});
  else if (sk == 1) fin(std::integral_constant<int, 1>{});
  else if (sk == 2) fin(std::integral_constant<int, 2>{});
  else fin(std::integral_constant<int, 3>{});
  ST2();
}

extern "C" void kernel_launch(void* const* d_in, const int* in_sizes, int n_in,
                              void* d_out, int out_size, void* d_ws, size_t ws_size,
                              hipStream_t stream) {
  const u16* xA = (const u16*)d_in[0]; const u16* kA = (const u16*)d_in[1]; const u16* vA = (const u16*)d_in[2];
  const u16* wqA = (const u16*)d_in[3]; const u16* woA = (const u16*)d_in[4];
  const u16* xB = (const u16*)d_in[5]; const u16* kB = (const u16*)d_in[6]; const u16* vB = (const u16*)d_in[7];
  const u16* wqB = (const u16*)d_in[8]; const u16* woB = (const u16*)d_in[9];
  u16* Og = (u16*)d_ws;
  float* part = (float*)((char*)d_ws + WS_PART_OFF);
  unsigned* cnt = (unsigned*)((char*)d_ws + WS_CNT_OFF);
  (void)hipFuncSetAttribute((const void*)k1_attn, hipFuncAttributeMaxDynamicSharedMemorySize, K1_LDS);
  (void)hipFuncSetAttribute((const void*)k2_oproj, hipFuncAttributeMaxDynamicSharedMemorySize, K2_LDS);
  hipLaunchKernelGGL(k1_attn, dim3(256), dim3(256), K1_LDS, stream, xA, kA, vA, wqA, xB, kB, vB, wqB, Og, cnt);
  hipLaunchKernelGGL(k2_oproj, dim3(256), dim3(576), K2_LDS, stream, Og, woA, woB, (u16*)d_out, part, cnt);
}
```

```cpp
#include <hip/hip_runtime.h>
#include <hip/hip_bf16.h>
#include <stdint.h>
#include <type_traits>

#define DI __device__ __forceinline__
typedef unsigned short u16;
typedef __attribute__((ext_vector_type(8))) __bf16 bf16x8;
typedef __attribute__((ext_vector_type(2))) __bf16 bf16x2;
typedef __attribute__((ext_vector_type(4))) short s16x4;
typedef __attribute__((ext_vector_type(8))) short s16x8;
typedef __attribute__((ext_vector_type(4))) float f32x4;
typedef __attribute__((ext_vector_type(2))) float f32x2;
typedef __attribute__((ext_vector_type(4))) unsigned u32x4;
typedef __attribute__((address_space(3))) void lds_void;
typedef __attribute__((address_space(3))) s16x4 lds_s16x4;

constexpr int NB = 8, SQ = 192, DM = 640, NH = 32, SK = 512, DH = 192;
constexpr int HD = NH * DH;
constexpr int MROWS = NB * SQ;
constexpr int SLOT = 24576;
constexpr int K1_LDS = 160 * 1024;

DI unsigned cvtpk(float lo, float hi) {
  f32x2 v = {lo, hi};
  bf16x2 b = __builtin_convertvector(v, bf16x2);
  return __builtin_bit_cast(unsigned, b);
}
DI f32x4 mfma16(bf16x8 a, bf16x8 b, f32x4 c) { return __builtin_amdgcn_mfma_f32_16x16x32_bf16(a, b, c, 0, 0, 0); }
DI void mfma_a(f32x4& c, bf16x8 a, bf16x8 b) { asm volatile("v_mfma_f32_16x16x32_bf16 %0, %1, %2, %0" : "+a"(c) : "v"(a), "v"(b)); }
DI void mfma_v(f32x4& c, bf16x8 a, bf16x8 b) { asm volatile("v_mfma_f32_16x16x32_bf16 %0, %1, %2, %0" : "+v"(c) : "v"(a), "a"(b)); }
DI void mfma_v0(f32x4& d, bf16x8 a, bf16x8 b, f32x4 c) { asm volatile("v_mfma_f32_16x16x32_bf16 %0, %1, %2, %3" : "=&v"(d) : "v"(a), "a"(b), "v"(c)); }
DI float fadd_s(float a, float b) { float r = a + b; asm("" : "+v"(r)); return r; }
#define MFMA_DRAIN() asm volatile("s_nop 7\n\ts_nop 7" ::: "memory")
DI bf16x8 lds_b128(const char* p) { return *reinterpret_cast<const bf16x8*>(p); }
DI bf16x8 lds_tr2(const char* p0, const char* p1) {
  s16x4 lo = __builtin_amdgcn_ds_read_tr16_b64_v4i16((lds_s16x4*)p0);
  s16x4 hi = __builtin_amdgcn_ds_read_tr16_b64_v4i16((lds_s16x4*)p1);
  s16x8 v = __builtin_shufflevector(lo, hi, 0, 1, 2, 3, 4, 5, 6, 7);
  return __builtin_bit_cast(bf16x8, v);
}
DI s16x4 lds_tr(const char* p) { return __builtin_amdgcn_ds_read_tr16_b64_v4i16((lds_s16x4*)p); }
DI bf16x8 cat8(s16x4 lo, s16x4 hi) { s16x8 v = __builtin_shufflevector(lo, hi, 0, 1, 2, 3, 4, 5, 6, 7); return __builtin_bit_cast(bf16x8, v); }
#define SB() __builtin_amdgcn_sched_barrier(0)
#define WAITV(n) asm volatile("s_waitcnt vmcnt(" #n ")" ::: "memory")
#define STAMP() do {} while (0)
#define PHASE_SYNC(n) do { STAMP(); asm volatile("s_waitcnt vmcnt(" #n ") lgkmcnt(0)" ::: "memory"); STAMP(); __builtin_amdgcn_s_barrier(); asm volatile("" ::: "memory"); STAMP(); } while (0)

DI u32x4 pair_fix(unsigned a0, unsigned a1, unsigned b0, unsigned b1) {
  auto r0 = __builtin_amdgcn_permlane32_swap(a0, b0, false, false);
  auto r1 = __builtin_amdgcn_permlane32_swap(a1, b1, false, false);
  auto s0 = __builtin_amdgcn_permlane16_swap(r0[0], r0[1], false, false);
  auto s1 = __builtin_amdgcn_permlane16_swap(r1[0], r1[1], false, false);
  u32x4 o = {s0[0], s1[0], s0[1], s1[1]};
  return o;
}

struct DmaOff { unsigned k[6], v[6], w[6], x[6]; };

DI void dma_tile(__amdgpu_buffer_rsrc_t rs, const unsigned (&vo)[6], unsigned soff, char* smem, int slot, int w) {
  const unsigned lb = (unsigned)(size_t)smem + (unsigned)(slot * SLOT + w * 6144);
#pragma unroll
  for (int j = 0; j < 6; ++j)
    asm volatile("s_mov_b32 m0, %0\n\ts_nop 0\n\tbuffer_load_dwordx4 %1, %2, %3 offen lds" :: "s"(lb + j * 1024u), "v"(vo[j]), "s"(rs), "s"(soff) : "memory");
}

struct DmaJob { __amdgpu_buffer_rsrc_t rs; unsigned soff, lb; unsigned vo[6]; int valid; };
DI DmaJob mkjob(__amdgpu_buffer_rsrc_t rs, const unsigned (&vo)[6], unsigned soff, char* smem, int slot, int w) {
  DmaJob j; j.rs = rs; j.soff = soff; j.lb = (unsigned)(size_t)smem + (unsigned)(slot * SLOT + w * 6144); j.valid = 1;
#pragma unroll
  for (int i = 0; i < 6; ++i) j.vo[i] = vo[i];
  return j;
}
DI void fixjob(DmaJob& j) { j.soff = __builtin_amdgcn_readfirstlane(j.soff); j.lb = __builtin_amdgcn_readfirstlane(j.lb); }
DI void dma_piece(const DmaJob& jb, int j) {
  asm volatile("s_mov_b32 m0, %0\n\ts_nop 0\n\tbuffer_load_dwordx4 %1, %2, %3 offen lds" :: "s"(jb.lb + j * 1024u), "v"(jb.vo[j]), "s"(jb.rs), "s"(jb.soff) : "memory");
}

__global__ __launch_bounds__(256, 1) void k1_attn(const u16* __restrict__ xA, const u16* __restrict__ kA, const u16* __restrict__ vA, const u16* __restrict__ wqA,
                                                  const u16* __restrict__ xB, const u16* __restrict__ kB, const u16* __restrict__ vB, const u16* __restrict__ wqB,
                                                  u16* __restrict__ Og, unsigned* __restrict__ cnt) {
  extern __shared__ __attribute__((aligned(16))) char smem[];
  const int tid = threadIdx.x, l = tid & 63;
  const int w = __builtin_amdgcn_readfirstlane(tid >> 6);
  const int r = l & 15, g = l >> 4, q4 = (l & 15) >> 2, p4 = l & 3;
  const int wg = blockIdx.x, xcd = wg & 7, idx = wg >> 3;
  const int head = xcd * 4 + (idx & 3), b = idx >> 2;

  if (blockIdx.x == 0 && tid < 64) cnt[tid * 32] = 0u;
  DmaOff D;
#pragma unroll
  for (int j = 0; j < 6; ++j) {
    const int p = (w * 6 + j) * 64 + l;
    const int row = p / 24, chp = p % 24;
    D.k[j] = row * 384 + ((chp & ~7) | ((chp & 7) ^ ((row >> 1) & 7))) * 16;
    D.v[j] = row * 384 + ((chp & ~7) | ((chp & 7) ^ (((row >> 1) & 3) << 1))) * 16;
    D.w[j] = row * 12288 + ((chp & ~7) | ((chp & 7) ^ ((((row >> 1) & 1) << 1) | (((row >> 3) & 1) << 2)))) * 16;
    const int rx = p >> 3, cx = p & 7;
    D.x[j] = rx * 1280 + (cx ^ ((rx >> 1) & 7)) * 16;
  }
  const int kb0 = r * 384 + ((g) ^ (r >> 1)) * 16;
  const int kb1 = r * 384 + ((4 + g) ^ (r >> 1)) * 16;
  int vbs[4], wbs[4];
  {
    const int fv = ((2 * g + (q4 >> 1)) & 3) << 1;
    const int fw = ((q4 >> 1) << 1) | ((g & 1) << 2);
#pragma unroll
    for (int c = 0; c < 4; ++c) {
      vbs[c] = (4 * g + q4) * 384 + (((2 * c) ^ fv) + (p4 >> 1)) * 16 + 8 * (p4 & 1);
      wbs[c] = (8 * g + q4) * 384 + (((2 * c) ^ fw) + (p4 >> 1)) * 16 + 8 * (p4 & 1);
    }
  }
  const int xb0 = (48 * w + r) * 128 + ((g) ^ (r >> 1)) * 16;
  const int xb1 = (48 * w + r) * 128 + ((4 + g) ^ (r >> 1)) * 16;

  const unsigned xoff = (unsigned)b * (SQ * DM * 2);
  const unsigned woff = (unsigned)head * (DH * 2);
  const unsigned kvoff = (unsigned)(b * NH + head) * (SK * DH * 2);

  __amdgpu_buffer_rsrc_t rX = __builtin_amdgcn_make_buffer_rsrc((void*)xA, 0, NB * SQ * DM * 2, 0x00020000);
  __amdgpu_buffer_rsrc_t rW = __builtin_amdgcn_make_buffer_rsrc((void*)wqA, 0, DM * HD * 2, 0x00020000);
  __amdgpu_buffer_rsrc_t rK = __builtin_amdgcn_make_buffer_rsrc((void*)kA, 0, NB * NH * SK * DH * 2, 0x00020000);
  __amdgpu_buffer_rsrc_t rV = __builtin_amdgcn_make_buffer_rsrc((void*)vA, 0, NB * NH * SK * DH * 2, 0x00020000);
  const __amdgpu_buffer_rsrc_t rXn = __builtin_amdgcn_make_buffer_rsrc((void*)xB, 0, NB * SQ * DM * 2, 0x00020000);
  const __amdgpu_buffer_rsrc_t rWn = __builtin_amdgcn_make_buffer_rsrc((void*)wqB, 0, DM * HD * 2, 0x00020000);
  const __amdgpu_buffer_rsrc_t rKn = __builtin_amdgcn_make_buffer_rsrc((void*)kB, 0, NB * NH * SK * DH * 2, 0x00020000);
  const __amdgpu_buffer_rsrc_t rVn = __builtin_amdgcn_make_buffer_rsrc((void*)vB, 0, NB * NH * SK * DH * 2, 0x00020000);

#define ISSUE_X(R, i, slot) dma_tile(R, D.x, xoff + (i) * 128u, smem, slot, w)
#define ISSUE_W(R, i, slot) dma_tile(R, D.w, woff + (i) * (64u * 12288u), smem, slot, w)
#define ISSUE_K(t, slot) dma_tile(rK, D.k, kvoff + (t) * 24576u, smem, slot, w)
#define ISSUE_V(t, slot) dma_tile(rV, D.v, kvoff + (t) * 24576u, smem, slot, w)

#define JOB_X(R, i, slot) mkjob(R, D.x, xoff + (i) * 128u, smem, slot, w)
#define JOB_W(R, i, slot) mkjob(R, D.w, woff + (i) * (64u * 12288u), smem, slot, w)
#define JOB_K(t, slot) mkjob(rK, D.k, kvoff + (t) * 24576u, smem, slot, w)
#define JOB_V(t, slot) mkjob(rV, D.v, kvoff + (t) * 24576u, smem, slot, w)
  DmaJob DUMMY = JOB_X(rX, 0, 0); DUMMY.lb = (unsigned)(size_t)smem + 6u * SLOT + (unsigned)w * 1024u;

  ISSUE_X(rX, 0, 0); ISSUE_W(rW, 0, 1); ISSUE_X(rX, 1, 2); ISSUE_W(rW, 1, 3); ISSUE_X(rX, 2, 4); ISSUE_W(rW, 2, 5);

  const float QSCALE = 0.07216878364870322f * 1.4426950408889634f;
  constexpr std::true_type T_{}; constexpr std::false_type F_{};

  for (int unit = 0; unit < 2; ++unit) {
    f32x4 acc[12][3];
#pragma unroll
    for (int c = 0; c < 12; ++c)
#pragma unroll
      for (int qt = 0; qt < 3; ++qt) { acc[c][qt] = f32x4{0.f, 0.f, 0.f, 0.f}; asm volatile("" : "+a"(acc[c][qt])); }

    {
      bf16x8 xf[2][3];
      s16x4 wlo[4], whi[4];
      auto waddr = [&](const char* pw, int i) { const int ks1 = i / 12, c1 = i % 12; return pw + wbs[c1 & 3] + (32 * ks1) * 384 + (c1 >> 2) * 128; };
      auto qphase = [&](int sx, int sxn, const DmaJob& j0, const DmaJob& j1, auto hasNext_, int syncn) {
        constexpr bool hasNext = decltype(hasNext_)::value;
        const char* px = smem + sx * SLOT;
        const char* pw = px + SLOT;
        const char* pxn = smem + sxn * SLOT;
        const char* pwn = pxn + SLOT;
#pragma unroll
        for (int i = 0; i < 24; ++i) {
          const int ks = i / 12, c = i % 12;
          const bf16x8 wf = cat8(wlo[i & 3], whi[i & 3]);
          if constexpr (hasNext) {
            if (i == 21) { if (syncn == 12) { PHASE_SYNC(12); } else if (syncn == 18) { PHASE_SYNC(18); } else { PHASE_SYNC(24); } }
          }
          const bool ld = (i + 3 < 24) || hasNext;
          const char* an = (i + 3 < 24) ? waddr(pw, i + 3) : waddr(pwn, i + 3 - 24);
          mfma_a(acc[c][0], wf, xf[ks][0]);
          if (ld) wlo[(i + 3) & 3] = lds_tr(an);
          SB();
          mfma_a(acc[c][1], wf, xf[ks][1]);
          if (ld) whi[(i + 3) & 3] = lds_tr(an + 4 * 384);
          SB();
          mfma_a(acc[c][2], wf, xf[ks][2]);
          if (i < 3) xf[1][i] = lds_b128(px + xb1 + i * 2048);
          else if (i >= 21) { if constexpr (hasNext) xf[0][i - 21] = lds_b128(pxn + xb0 + (i - 21) * 2048); }
          else if (i < 15) { if (i & 1) dma_piece(j0, (i - 3) >> 1); }
          else dma_piece(j1, i - 15);
          SB();
        }
      };
      if (unit == 0) { PHASE_SYNC(24); } else { PHASE_SYNC(36); }
#pragma unroll
      for (int i = 0; i < 3; ++i) xf[0][i] = lds_b128(smem + xb0 + i * 2048);
#pragma unroll
      for (int i = 0; i < 3; ++i) { const char* a = waddr(smem + SLOT, i); wlo[i] = lds_tr(a); whi[i] = lds_tr(a + 4 * 384); }
      int sx = 0;
      for (int i = 0; i < 9; ++i) {
        const int sp = (sx == 0) ? 4 : sx - 2;
        const int sxn = (sx == 4) ? 0 : sx + 2;
        DmaJob j0, j1;
        if (i == 0) { j0 = JOB_W(rW, 2, 5); if (unit == 0) j0 = DUMMY; j1 = DUMMY; }
        else if (i < 8) { j0 = JOB_X(rX, i + 2, sp); j1 = JOB_W(rW, i + 2, sp + 1); }
        else { j0 = JOB_K(0, 2); j1 = JOB_K(1, 3); }
        fixjob(j0); fixjob(j1);
        qphase(sx, sxn, j0, j1, T_, i == 0 ? 24 : (i == 1 ? 18 : 12));
        sx = sxn;
      }
      qphase(0, 0, JOB_V(0, 4), JOB_K(2, 5), F_, 0);
    }

    MFMA_DRAIN();
    bf16x8 qf[3][6];
#pragma unroll
    for (int qt = 0; qt < 3; ++qt)
#pragma unroll
      for (int s = 0; s < 6; ++s) {
        asm volatile("" : "+a"(acc[2 * s][qt]), "+a"(acc[2 * s + 1][qt]));
        const f32x4 A = acc[2 * s][qt] * QSCALE, B = acc[2 * s + 1][qt] * QSCALE;
        u32x4 o = pair_fix(cvtpk(A[0], A[1]), cvtpk(A[2], A[3]), cvtpk(B[0], B[1]), cvtpk(B[2], B[3]));
        qf[qt][s] = __builtin_bit_cast(bf16x8, o);
        asm volatile("" : "+a"(qf[qt][s]));
        SB();
      }
#pragma unroll
    for (int c = 0; c < 12; ++c)
#pragma unroll
      for (int qt = 0; qt < 3; ++qt) { acc[c][qt] = f32x4{0.f, 0.f, 0.f, 0.f}; asm volatile("" : "+a"(acc[c][qt])); }

    float mref[3], lsum[3] = {0.f, 0.f, 0.f};
    f32x4 negm[3] = {f32x4{0.f, 0.f, 0.f, 0.f}, f32x4{0.f, 0.f, 0.f, 0.f}, f32x4{0.f, 0.f, 0.f, 0.f}};
    f32x4 Sc[2][3];
    bf16x8 Pp[3];
    bf16x8 kf[3];
    s16x4 vlo[3], vhi[3];

    auto sm = [&](const f32x4 (&S)[2][3], bf16x8 (&P)[3], float (&ps)[3]) {
#pragma unroll
      for (int qt = 0; qt < 3; ++qt) {
        float e[8];
#pragma unroll
        for (int i = 0; i < 8; ++i) e[i] = __builtin_amdgcn_exp2f(S[i >> 2][qt][i & 3]);
        ps[qt] = ((e[0] + e[1]) + (e[2] + e[3])) + ((e[4] + e[5]) + (e[6] + e[7]));
        u32x4 o = {cvtpk(e[0], e[1]), cvtpk(e[2], e[3]), cvtpk(e[4], e[5]), cvtpk(e[6], e[7])};
        P[qt] = __builtin_bit_cast(bf16x8, o);
      }
    };
    auto rowmax = [&](const f32x4 (&S)[2][3], int qt) {
      float mx = fmaxf(fmaxf(fmaxf(S[0][qt][0], S[0][qt][1]), fmaxf(S[0][qt][2], S[0][qt][3])),
                       fmaxf(fmaxf(S[1][qt][0], S[1][qt][1]), fmaxf(S[1][qt][2], S[1][qt][3])));
      mx = fmaxf(mx, __shfl_xor(mx, 16));
      mx = fmaxf(mx, __shfl_xor(mx, 32));
      return mx;
    };
    auto kaddr = [&](const char* pk, int i) { const int s1 = i >> 1, k21 = i & 1; return pk + ((s1 & 1) ? kb1 : kb0) + k21 * 6144 + (s1 >> 1) * 128; };
    auto vaddr = [&](const char* pv, int c1) { return pv + vbs[c1 & 3] + (c1 >> 2) * 128; };
    auto kbase = [&](int slot, int hb) { return (const char*)smem + slot * SLOT + hb * (2 * 6144); };
    auto vbase = [&](int slot, int ks) { return (const char*)smem + slot * SLOT + (32 * ks) * 384; };
    auto step = [&](auto doQK_, const char* pk, auto doPV_, const char* pvv, auto doSM_, const DmaJob& jb,
                    auto nQK_, const char* pkn, auto nPV_, const char* pvn, auto hasSync_, int syncn) {
      constexpr bool doQK = decltype(doQK_)::value, doPV = decltype(doPV_)::value, doSM = decltype(doSM_)::value;
      constexpr bool nQK = decltype(nQK_)::value, nPV = decltype(nPV_)::value, hasSync = decltype(hasSync_)::value;
      f32x4 Sn[2][3];
      u32x4 Pn[3];
      float ps[3] = {0.f, 0.f, 0.f};
      float ea = 0.f, eb = 0.f;
#pragma unroll
      for (int i = 0; i < 12; ++i) {
        const int s = i >> 1, k2 = i & 1, ri = i % 3, rn = (i + 2) % 3;
        if constexpr (hasSync) { if (i == 10) { if (syncn == 12) { PHASE_SYNC(12); } else { PHASE_SYNC(18); } } }
        const bool in = (i + 2 < 12);
        const bool ldK = in ? doQK : nQK, ldV = in ? doPV : nPV;
        const char* ka = in ? kaddr(pk, i + 2) : kaddr(pkn, i + 2 - 12);
        const char* va = in ? vaddr(pvv, i + 2) : vaddr(pvn, i + 2 - 12);
        const bf16x8 kc = kf[ri];
        const bf16x8 vc = cat8(vlo[ri], vhi[ri]);
        const int pq = (i - 1) >> 2, pj = (i - 1) & 3;
        const int eq = i >> 2, e0 = (i & 3) * 2;
        if constexpr (doQK) { if (s == 0) mfma_v0(Sn[k2][0], kc, qf[0][s], negm[0]); else mfma_v(Sn[k2][0], kc, qf[0][s]); }
        if (ldV) vlo[rn] = lds_tr(va);
        if constexpr (doSM) { if (i > 0) { unsigned pk2 = cvtpk(ea, eb); asm volatile("" : "+v"(pk2)); Pn[pq][pj] = pk2; } }
        SB();
        if constexpr (doQK) { if (s == 0) mfma_v0(Sn[k2][1], kc, qf[1][s], negm[1]); else mfma_v(Sn[k2][1], kc, qf[1][s]); }
        if (ldV) vhi[rn] = lds_tr(va + 16 * 384);
        if constexpr (doSM) { if (i > 0) { if (pj == 0) ps[pq] = fadd_s(ea, eb); else ps[pq] = fadd_s(ps[pq], ea); } }
        SB();
        if constexpr (doQK) { if (s == 0) mfma_v0(Sn[k2][2], kc, qf[2][s], negm[2]); else mfma_v(Sn[k2][2], kc, qf[2][s]); }
        if (ldK) kf[rn] = lds_b128(ka);
        if constexpr (doSM) { if (i > 0) { if (pj != 0) ps[pq] = fadd_s(ps[pq], eb); } }
        SB();
        if constexpr (doPV) mfma_a(acc[i][0], vc, Pp[0]);
        if constexpr (doSM) { ea = __builtin_amdgcn_exp2f(Sc[e0 >> 2][eq][e0 & 3]); asm volatile("" : "+v"(ea)); }
        SB();
        if constexpr (doPV) mfma_a(acc[i][1], vc, Pp[1]);
        if constexpr (doSM) { eb = __builtin_amdgcn_exp2f(Sc[e0 >> 2][eq][(e0 & 3) + 1]); asm volatile("" : "+v"(eb)); }
        SB();
        if constexpr (doPV) mfma_a(acc[i][2], vc, Pp[2]);
        if (i < 10) { if ((i & 1) == 0) dma_piece(jb, i >> 1); else if (i == 9) dma_piece(jb, 5); }
        if constexpr (doQK) { if (i == 2) asm volatile("" :: "v"(negm[0]), "v"(negm[1]), "v"(negm[2])); }
        SB();
      }
      if constexpr (doSM) {
        Pn[2][3] = cvtpk(ea, eb);
        ps[2] = fadd_s(ps[2], ea); ps[2] = fadd_s(ps[2], eb);
        const bool bad = !(ps[0] <= 1.0e12f) || !(ps[1] <= 1.0e12f) || !(ps[2] <= 1.0e12f);
        bf16x8 Pb[3];
#pragma unroll
        for (int qt = 0; qt < 3; ++qt) Pb[qt] = __builtin_bit_cast(bf16x8, Pn[qt]);
        if (__builtin_expect(__any(bad), 0)) {
          MFMA_DRAIN();
          if constexpr (doQK) asm volatile("" : "+v"(Sn[0][0]), "+v"(Sn[0][1]), "+v"(Sn[0][2]), "+v"(Sn[1][0]), "+v"(Sn[1][1]), "+v"(Sn[1][2]));
#pragma unroll
          for (int qt = 0; qt < 3; ++qt) {
            const float d = fmaxf(rowmax(Sc, qt), 0.f);
            const float f = __builtin_amdgcn_exp2f(-d);
            mref[qt] += d;
            negm[qt] = f32x4{-mref[qt], -mref[qt], -mref[qt], -mref[qt]};
            asm volatile("s_nop 1" : "+v"(negm[qt]));
            lsum[qt] *= f;
#pragma unroll
            for (int c = 0; c < 12; ++c) { asm volatile("" : "+a"(acc[c][qt])); acc[c][qt] *= f; asm volatile("" : "+a"(acc[c][qt])); SB(); }
#pragma unroll
            for (int k2 = 0; k2 < 2; ++k2) { Sc[k2][qt] -= d; if constexpr (doQK) Sn[k2][qt] -= d; }
          }
          sm(Sc, Pb, ps);
        }
#pragma unroll
        for (int qt = 0; qt < 3; ++qt) { lsum[qt] += ps[qt]; Pp[qt] = Pb[qt]; asm volatile("s_nop 0" : "+v"(Pp[qt])); }
      }
      if constexpr (doQK) {
#pragma unroll
        for (int qt = 0; qt < 3; ++qt) { Sc[0][qt] = Sn[0][qt]; Sc[1][qt] = Sn[1][qt]; }
      }
    };

    PHASE_SYNC(18);
    kf[0] = lds_b128(kaddr(kbase(2, 0), 0)); kf[1] = lds_b128(kaddr(kbase(2, 0), 1));
    step(T_, kbase(2, 0), F_, smem, F_, JOB_V(1, 0), T_, kbase(2, 1), F_, smem, F_, 0);
    asm volatile("s_nop 7\n\ts_nop 7" : "+v"(Sc[0][0]), "+v"(Sc[0][1]), "+v"(Sc[0][2]), "+v"(Sc[1][0]), "+v"(Sc[1][1]), "+v"(Sc[1][2]));
#pragma unroll
    for (int qt = 0; qt < 3; ++qt) {
      const float mx = rowmax(Sc, qt);
      mref[qt] = mx;
      negm[qt] = f32x4{-mx, -mx, -mx, -mx};
      asm volatile("s_nop 1" : "+v"(negm[qt]));
      Sc[0][qt] -= mx; Sc[1][qt] -= mx;
    }
    step(T_, kbase(2, 1), F_, smem, T_, JOB_K(3, 1), T_, kbase(3, 0), T_, vbase(4, 0), T_, 18);
    {
      int ksl = 3;
      for (int t = 1; t < 7; ++t) {
        const int vsl = (ksl == 5) ? 0 : ksl + 1;
        const int kfree = (ksl == 1) ? 5 : ksl - 2;
        const int vfree = (kfree == 5) ? 0 : kfree + 1;
        const int ksn = (ksl == 5) ? 1 : ksl + 2, vsn = (ksn == 5) ? 0 : ksn + 1;
        DmaJob j0, j1;
        if (t == 1) { j0 = JOB_V(2, 2); j1 = DUMMY; }
        else if (t < 6) { j0 = JOB_K(t + 2, kfree); j1 = JOB_V(t + 1, vfree); }
        else { j0 = JOB_V(7, 5); j1 = JOB_X(rXn, 0, 0); if (unit) j1 = DUMMY; }
        fixjob(j0); fixjob(j1);
        step(T_, kbase(ksl, 0), T_, vbase(vsl, 0), T_, j0, T_, kbase(ksl, 1), T_, vbase(vsl, 1), F_, 0);
        step(T_, kbase(ksl, 1), T_, vbase(vsl, 1), T_, j1, T_, kbase(ksn, 0), T_, vbase(vsn, 0), T_, t < 3 ? 18 : 12);
        ksl = ksn;
      }
    }
    {
      DmaJob j0 = JOB_W(rWn, 0, 1), j1 = JOB_X(rXn, 1, 2), j2 = JOB_W(rWn, 1, 3), j3 = JOB_X(rXn, 2, 4);
      if (unit) { j0 = DUMMY; j1 = DUMMY; j2 = DUMMY; j3 = DUMMY; }
      fixjob(j0); fixjob(j1); fixjob(j2); fixjob(j3);
      step(T_, kbase(3, 0), T_, vbase(4, 0), T_, j0, T_, kbase(3, 1), T_, vbase(4, 1), F_, 0);
      step(T_, kbase(3, 1), T_, vbase(4, 1), T_, j1, F_, smem, T_, vbase(5, 0), T_, 18);
      step(F_, smem, T_, vbase(5, 0), T_, j2, F_, smem, T_, vbase(5, 1), F_, 0);
      step(F_, smem, T_, vbase(5, 1), F_, j3, F_, smem, F_, smem, F_, 0);
    }

    MFMA_DRAIN();
    {
      u16* orow = Og + (size_t)unit * MROWS * HD + ((size_t)(3 * head) * MROWS + (b * SQ + 48 * w + r)) * 64 + 8 * g;
#pragma unroll
      for (int qt = 0; qt < 3; ++qt) {
        float lt = lsum[qt];
        lt += __shfl_xor(lt, 16);
        lt += __shfl_xor(lt, 32);
        const float inv = 1.0f / lt;
#pragma unroll
        for (int s = 0; s < 6; ++s) {
          asm volatile("" : "+a"(acc[2 * s][qt]), "+a"(acc[2 * s + 1][qt]));
          const f32x4 A = acc[2 * s][qt] * inv, B = acc[2 * s + 1][qt] * inv;
          u32x4 o = pair_fix(cvtpk(A[0], A[1]), cvtpk(A[2], A[3]), cvtpk(B[0], B[1]), cvtpk(B[2], B[3]));
          *reinterpret_cast<u32x4*>(orow + (size_t)(s >> 1) * MROWS * 64 + qt * 16 * 64 + 32 * (s & 1)) = o;
          SB();
        }
      }
    }
    STAMP();
    rX = rXn; rW = rWn; rK = rKn; rV = rVn;
  }
  asm volatile("s_waitcnt vmcnt(0)" ::: "memory");
}

constexpr int K2_STAGE = 24576 + 20480;
constexpr int K2_LDS = 3 * K2_STAGE;
#define K2_PF 5
constexpr size_t WS_O_BYTES = (size_t)2 * MROWS * HD * 2;
constexpr size_t WS_PART_OFF = WS_O_BYTES;
constexpr size_t WS_PART_BYTES = (size_t)64 * 4 * 8 * 15360;
constexpr size_t WS_CNT_OFF = WS_PART_OFF + WS_PART_BYTES;

__global__ __launch_bounds__(576, 2) void k2_oproj(const u16* __restrict__ Og, const u16* __restrict__ woA, const u16* __restrict__ woB,
                                                   u16* __restrict__ out, float* __restrict__ part, unsigned* __restrict__ cnt) {
  extern __shared__ __attribute__((aligned(16))) char smem[];
  const int tid = threadIdx.x, l = tid & 63;
  const int w = __builtin_amdgcn_readfirstlane(tid >> 6);
  const int r = l & 15, g = l >> 4, q4 = (l & 15) >> 2, p4 = l & 3;
  const int mw = w >> 1, nw = w & 1;
  const int wg = blockIdx.x, xcd = wg & 7, idx = wg >> 3;
  const int br = xcd >> 2, sk = xcd & 3, mt = idx >> 2, nt = idx & 3;
  const int tl = br * 32 + idx;
  const u16* wo = br ? woB : woA;
  const __amdgpu_buffer_rsrc_t rO = __builtin_amdgcn_make_buffer_rsrc((void*)(Og + (size_t)br * MROWS * HD), 0, MROWS * HD * 2, 0x00020000);
  const __amdgpu_buffer_rsrc_t rW = __builtin_amdgcn_make_buffer_rsrc((void*)wo, 0, HD * DM * 2, 0x00020000);
  const unsigned ooff = ((unsigned)(sk * 24) * MROWS + (unsigned)(mt * 192)) * 128u;
  const unsigned woff = (unsigned)(sk * 1536) * (DM * 2) + (unsigned)nt * 320;
  if (w == 8) {
    const unsigned to = (l < 48) ? (unsigned)(nt * 48 + l) * 128u : 0x7fffffffu;
    const unsigned tw = (l < 24) ? (unsigned)(mt * 8 + l / 3) * (DM * 2) + (unsigned)(l % 3) * 128u : 0x7fffffffu;
    unsigned junk = 0;
    auto tch = [&](int st) {
      junk ^= __builtin_amdgcn_raw_buffer_load_b32(rO, to, ooff + st * (MROWS * 128u), 0);
      junk ^= __builtin_amdgcn_raw_buffer_load_b32(rW, tw, (woff & ~127u) + st * (64u * DM * 2), 0);
    };
#pragma unroll
    for (int st = 2; st < 2 + K2_PF; ++st) tch(st);
    __builtin_amdgcn_s_barrier();
    for (int st = 0; st < 24; ++st) {
      if (st + 2 + K2_PF < 24) tch(st + 2 + K2_PF);
      __builtin_amdgcn_s_barrier();
    }
    if (junk == 0x9e3779b9u) cnt[tl * 32 + 1] = junk;
    return;
  }
  unsigned vo[3], vw[3], lo_[3], lw_[3];
#pragma unroll
  for (int j = 0; j < 3; ++j) {
    const int pi = w + 8 * j;
    const int p = pi * 64 + l, rx = p >> 3, cx = p & 7;
    vo[j] = rx * 128 + (cx ^ ((rx >> 1) & 7)) * 16;
    lo_[j] = pi * 1024;
    int wi = w + 8 * j; if (wi >= 20) wi -= 4;
    const int pw = wi * 64 + l, row = pw / 20, ch = pw % 20;
    vw[j] = row * (DM * 2) + (ch ^ (((row >> 3) & 1) << 1)) * 16;
    lw_[j] = 24576 + wi * 1024;
  }
  const unsigned lbase = (unsigned)(size_t)smem;
  auto issue1 = [&](int st, int buf, int j) {
    const unsigned lb = lbase + buf * K2_STAGE;
    if (j & 1) asm volatile("s_mov_b32 m0, %0\n\ts_nop 0\n\tbuffer_load_dwordx4 %1, %2, %3 offen lds" :: "s"(lb + lw_[j >> 1]), "v"(vw[j >> 1]), "s"(rW), "s"(woff + st * (64u * DM * 2)) : "memory");
    else       asm volatile("s_mov_b32 m0, %0\n\ts_nop 0\n\tbuffer_load_dwordx4 %1, %2, %3 offen lds" :: "s"(lb + lo_[j >> 1]), "v"(vo[j >> 1]), "s"(rO), "s"(ooff + st * (MROWS * 128u)) : "memory");
  };
  const int ob0 = (48 * mw + r) * 128 + ((g) ^ (r >> 1)) * 16;
  const int ob1 = (48 * mw + r) * 128 + ((4 + g) ^ (r >> 1)) * 16;
  int wb[5];
#pragma unroll
  for (int c = 0; c < 5; ++c) wb[c] = 24576 + (8 * g + q4) * 320 + (p4 >> 1) * 16 + 8 * (p4 & 1) + (((5 * nw + c) ^ (g & 1)) * 32);
#define ST2() do {} while (0)
  f32x4 acc[5][3] = {};
  bf16x8 of0[3], wf0[5], of1[3], wf1[5];
  auto ldfr = [&](bf16x8 (&of)[3], bf16x8 (&wf)[5], const char* pb, int ks) {
#pragma unroll
    for (int m = 0; m < 3; ++m) of[m] = lds_b128(pb + (ks ? ob1 : ob0) + m * 2048);
#pragma unroll
    for (int c = 0; c < 5; ++c) { const char* a0 = pb + wb[c] + (32 * ks) * 320; wf[c] = cat8(lds_tr(a0), lds_tr(a0 + 4 * 320)); }
  };
  ST2();
#pragma unroll
  for (int j = 0; j < 6; ++j) issue1(0, 0, j);
#pragma unroll
  for (int j = 0; j < 6; ++j) issue1(1, 1, j);
  asm volatile("s_waitcnt vmcnt(6)" ::: "memory");
  __builtin_amdgcn_s_barrier();
  asm volatile("" ::: "memory");
  ldfr(of0, wf0, smem, 0);
  int buf = 0;
  for (int st = 0; st < 24; ++st) {
    const char* pb = smem + buf * K2_STAGE;
    const int bn = (buf == 2) ? 0 : buf + 1, bp = (buf == 0) ? 2 : buf - 1;
    ST2();
    ldfr(of1, wf1, pb, 1);
    SB();
#pragma unroll
    for (int c = 0; c < 5; ++c) {
#pragma unroll
      for (int m = 0; m < 3; ++m) acc[c][m] = mfma16(wf0[c], of0[m], acc[c][m]);
      if (st + 2 < 24) { issue1(st + 2, bp, c); if (c == 4) issue1(st + 2, bp, 5); }
      SB();
    }
    ST2();
    if (st < 22) { asm volatile("s_waitcnt vmcnt(6) lgkmcnt(0)" ::: "memory"); } else { asm volatile("s_waitcnt vmcnt(0) lgkmcnt(0)" ::: "memory"); }
    __builtin_amdgcn_s_barrier();
    asm volatile("" ::: "memory");
    ST2();
    if (st + 1 < 24) ldfr(of0, wf0, smem + bn * K2_STAGE, 0);
    SB();
#pragma unroll
    for (int c = 0; c < 5; ++c)
#pragma unroll
      for (int m = 0; m < 3; ++m) acc[c][m] = mfma16(wf1[c], of1[m], acc[c][m]);
    buf = bn;
  }
  ST2();
  float* slab = part + ((size_t)(tl * 4) * 8) * 3840;
  const __amdgpu_buffer_rsrc_t rP = __builtin_amdgcn_make_buffer_rsrc((void*)slab, 0, 4 * 8 * 15360, 0x00020000);
#pragma unroll
  for (int c = 0; c < 5; ++c)
#pragma unroll
    for (int m = 0; m < 3; ++m) {
      const int owner = (c == 4) ? 3 : m;
      if (owner != sk) __builtin_amdgcn_raw_buffer_store_b128(__builtin_bit_cast(u32x4, acc[c][m]), rP, l * 16, (unsigned)((sk * 8 + w) * 15360 + (c * 3 + m) * 1024), 16);
    }
  asm volatile("s_waitcnt vmcnt(0)" ::: "memory");
  __syncthreads();
  if (tid == 0) __hip_atomic_fetch_add(cnt + tl * 32, 1u, __ATOMIC_RELAXED, __HIP_MEMORY_SCOPE_AGENT);
  ST2();
  if (w == 0) {
    unsigned spins = 0;
    while (__hip_atomic_load(cnt + tl * 32, __ATOMIC_RELAXED, __HIP_MEMORY_SCOPE_AGENT) < 4u) { __builtin_amdgcn_s_sleep(1); if (++spins > (1u << 24)) break; }
  }
  __syncthreads();
  ST2();
  u16* orow = out + (size_t)(mt * 192 + 48 * mw + r) * (2 * DM) + br * DM + nt * 160 + 80 * nw;
  auto fin = [&](auto J_) {
    constexpr int J = decltype(J_)::value;
    constexpr int NT = (J == 3) ? 3 : 4;
    u32x4 v[3][NT];
#pragma unroll
    for (int s2 = 0; s2 < 3; ++s2) {
      const int src = s2 + (s2 >= J ? 1 : 0);
#pragma unroll
      for (int i = 0; i < NT; ++i) {
        const int ti = (J == 3) ? (12 + i) : (i * 3 + J);
        v[s2][i] = __builtin_amdgcn_raw_buffer_load_b128(rP, l * 16, (unsigned)((src * 8 + w) * 15360 + ti * 1024), 16);
      }
    }
    f32x4 sum[NT];
#pragma unroll
    for (int i = 0; i < NT; ++i) {
      const f32x4 own = (J == 3) ? acc[4][i] : acc[i][J];
      f32x4 t[4];
#pragma unroll
      for (int s4 = 0; s4 < 4; ++s4) t[s4] = (s4 == J) ? own : __builtin_bit_cast(f32x4, v[s4 - (s4 > J ? 1 : 0)][i]);
      sum[i] = (t[0] + t[1]) + (t[2] + t[3]);
    }
    if constexpr (J == 3) {
#pragma unroll
      for (int m = 0; m < 3; ++m) {
        uint2 e2 = make_uint2(cvtpk(sum[m][0], sum[m][1]), cvtpk(sum[m][2], sum[m][3]));
        *reinterpret_cast<uint2*>(orow + (size_t)m * 16 * (2 * DM) + 64 + 4 * g) = e2;
      }
    } else {
#pragma unroll
      for (int c = 0; c < 4; c += 2) {
        const f32x4 A = sum[c], B = sum[c + 1];
        u32x4 o = pair_fix(cvtpk(A[0], A[1]), cvtpk(A[2], A[3]), cvtpk(B[0], B[1]), cvtpk(B[2], B[3]));
        *reinterpret_cast<u32x4*>(orow + (size_t)J * 16 * (2 * DM) + 16 * c + 8 * g) = o;
      }
    }
  };
  if (sk == 0) fin(std::integral_constant<int, 0>{});
  else if (sk == 1) fin(std::integral_constant<int, 1>{});
  else if (sk == 2) fin(std::integral_constant<int, 2>{});
  else fin(std::integral_constant<int, 3>{});
  ST2();
}

extern "C" void kernel_launch(void* const* d_in, const int* in_sizes, int n_in,
                              void* d_out, int out_size, void* d_ws, size_t ws_size,
                              hipStream_t stream) {
  const u16* xA = (const u16*)d_in[0]; const u16* kA = (const u16*)d_in[1]; const u16* vA = (const u16*)d_in[2];
  const u16* wqA = (const u16*)d_in[3]; const u16* woA = (const u16*)d_in[4];
  const u16* xB = (const u16*)d_in[5]; const u16* kB = (const u16*)d_in[6]; const u16* vB = (const u16*)d_in[7];
  const u16* wqB = (const u16*)d_in[8]; const u16* woB = (const u16*)d_in[9];
  u16* Og = (u16*)d_ws;
  float* part = (float*)((char*)d_ws + WS_PART_OFF);
  unsigned* cnt = (unsigned*)((char*)d_ws + WS_CNT_OFF);
  (void)hipFuncSetAttribute((const void*)k1_attn, hipFuncAttributeMaxDynamicSharedMemorySize, K1_LDS);
  (void)hipFuncSetAttribute((const void*)k2_oproj, hipFuncAttributeMaxDynamicSharedMemorySize, K2_LDS);
  hipLaunchKernelGGL(k1_attn, dim3(256), dim3(256), K1_LDS, stream, xA, kA, vA, wqA, xB, kB, vB, wqB, Og, cnt);
  hipLaunchKernelGGL(k2_oproj, dim3(256), dim3(576), K2_LDS, stream, Og, woA, woB, (u16*)d_out, part, cnt);
}
```

```cpp
#include <hip/hip_runtime.h>
#include <hip/hip_bf16.h>
#include <stdint.h>
#include <type_traits>

#define DI __device__ __forceinline__
typedef unsigned short u16;
typedef __attribute__((ext_vector_type(8))) __bf16 bf16x8;
typedef __attribute__((ext_vector_type(2))) __bf16 bf16x2;
typedef __attribute__((ext_vector_type(4))) short s16x4;
typedef __attribute__((ext_vector_type(8))) short s16x8;
typedef __attribute__((ext_vector_type(4))) float f32x4;
typedef __attribute__((ext_vector_type(2))) float f32x2;
typedef __attribute__((ext_vector_type(4))) unsigned u32x4;
typedef __attribute__((address_space(3))) void lds_void;
typedef __attribute__((address_space(3))) s16x4 lds_s16x4;

constexpr int NB = 8, SQ = 192, DM = 640, NH = 32, SK = 512, DH = 192;
constexpr int HD = NH * DH;
constexpr int MROWS = NB * SQ;
constexpr int SLOT = 24576;
constexpr int K1_LDS = 160 * 1024;

DI unsigned cvtpk(float lo, float hi) {
  f32x2 v = {lo, hi};
  bf16x2 b = __builtin_convertvector(v, bf16x2);
  return __builtin_bit_cast(unsigned, b);
}
DI f32x4 mfma16(bf16x8 a, bf16x8 b, f32x4 c) { return __builtin_amdgcn_mfma_f32_16x16x32_bf16(a, b, c, 0, 0, 0); }
DI void mfma_a(f32x4& c, bf16x8 a, bf16x8 b) { asm volatile("v_mfma_f32_16x16x32_bf16 %0, %1, %2, %0" : "+a"(c) : "v"(a), "v"(b)); }
DI void mfma_v(f32x4& c, bf16x8 a, bf16x8 b) { asm volatile("v_mfma_f32_16x16x32_bf16 %0, %1, %2, %0" : "+v"(c) : "v"(a), "a"(b)); }
DI void mfma_vv(f32x4& c, bf16x8 a, bf16x8 b) { asm volatile("v_mfma_f32_16x16x32_bf16 %0, %1, %2, %0" : "+v"(c) : "v"(a), "v"(b)); }
DI void mfma_v0(f32x4& d, bf16x8 a, bf16x8 b, f32x4 c) { asm volatile("v_mfma_f32_16x16x32_bf16 %0, %1, %2, %3" : "=&v"(d) : "v"(a), "a"(b), "v"(c)); }
DI float fadd_s(float a, float b) { float r = a + b; asm("" : "+v"(r)); return r; }
#define MFMA_DRAIN() asm volatile("s_nop 7\n\ts_nop 7" ::: "memory")
DI bf16x8 lds_b128(const char* p) { return *reinterpret_cast<const bf16x8*>(p); }
DI bf16x8 lds_tr2(const char* p0, const char* p1) {
  s16x4 lo = __builtin_amdgcn_ds_read_tr16_b64_v4i16((lds_s16x4*)p0);
  s16x4 hi = __builtin_amdgcn_ds_read_tr16_b64_v4i16((lds_s16x4*)p1);
  s16x8 v = __builtin_shufflevector(lo, hi, 0, 1, 2, 3, 4, 5, 6, 7);
  return __builtin_bit_cast(bf16x8, v);
}
DI s16x4 lds_tr(const char* p) { return __builtin_amdgcn_ds_read_tr16_b64_v4i16((lds_s16x4*)p); }
DI bf16x8 cat8(s16x4 lo, s16x4 hi) { s16x8 v = __builtin_shufflevector(lo, hi, 0, 1, 2, 3, 4, 5, 6, 7); return __builtin_bit_cast(bf16x8, v); }
#define SB() __builtin_amdgcn_sched_barrier(0)
#define WAITV(n) asm volatile("s_waitcnt vmcnt(" #n ")" ::: "memory")
#define STAMP() do {} while (0)
#define PHASE_SYNC(n) do { STAMP(); asm volatile("s_waitcnt vmcnt(" #n ") lgkmcnt(0)" ::: "memory"); STAMP(); __builtin_amdgcn_s_barrier(); asm volatile("" ::: "memory"); STAMP(); } while (0)

DI u32x4 pair_fix(unsigned a0, unsigned a1, unsigned b0, unsigned b1) {
  auto r0 = __builtin_amdgcn_permlane32_swap(a0, b0, false, false);
  auto r1 = __builtin_amdgcn_permlane32_swap(a1, b1, false, false);
  auto s0 = __builtin_amdgcn_permlane16_swap(r0[0], r0[1], false, false);
  auto s1 = __builtin_amdgcn_permlane16_swap(r1[0], r1[1], false, false);
  u32x4 o = {s0[0], s1[0], s0[1], s1[1]};
  return o;
}

struct DmaOff { unsigned k[6], v[6], w[6], x[6]; };

DI void dma_tile(__amdgpu_buffer_rsrc_t rs, const unsigned (&vo)[6], unsigned soff, char* smem, int slot, int w) {
  const unsigned lb = (unsigned)(size_t)smem + (unsigned)(slot * SLOT + w * 6144);
#pragma unroll
  for (int j = 0; j < 6; ++j)
    asm volatile("s_mov_b32 m0, %0\n\ts_nop 0\n\tbuffer_load_dwordx4 %1, %2, %3 offen lds" :: "s"(lb + j * 1024u), "v"(vo[j]), "s"(rs), "s"(soff) : "memory");
}

struct DmaJob { __amdgpu_buffer_rsrc_t rs; unsigned soff, lb; unsigned vo[6]; int valid; };
DI DmaJob mkjob(__amdgpu_buffer_rsrc_t rs, const unsigned (&vo)[6], unsigned soff, char* smem, int slot, int w) {
  DmaJob j; j.rs = rs; j.soff = soff; j.lb = (unsigned)(size_t)smem + (unsigned)(slot * SLOT + w * 6144); j.valid = 1;
#pragma unroll
  for (int i = 0; i < 6; ++i) j.vo[i] = vo[i];
  return j;
}
DI void fixjob(DmaJob& j) { j.soff = __builtin_amdgcn_readfirstlane(j.soff); j.lb = __builtin_amdgcn_readfirstlane(j.lb); }
DI void dma_piece(const DmaJob& jb, int j) {
  asm volatile("s_mov_b32 m0, %0\n\ts_nop 0\n\tbuffer_load_dwordx4 %1, %2, %3 offen lds" :: "s"(jb.lb + j * 1024u), "v"(jb.vo[j]), "s"(jb.rs), "s"(jb.soff) : "memory");
}

__global__ __launch_bounds__(256, 1) void k1_attn(const u16* __restrict__ xA, const u16* __restrict__ kA, const u16* __restrict__ vA, const u16* __restrict__ wqA,
                                                  const u16* __restrict__ xB, const u16* __restrict__ kB, const u16* __restrict__ vB, const u16* __restrict__ wqB,
                                                  u16* __restrict__ Og, unsigned* __restrict__ cnt) {
  extern __shared__ __attribute__((aligned(16))) char smem[];
  const int tid = threadIdx.x, l = tid & 63;
  const int w = __builtin_amdgcn_readfirstlane(tid >> 6);
  const int r = l & 15, g = l >> 4, q4 = (l & 15) >> 2, p4 = l & 3;
  const int wg = blockIdx.x, xcd = wg & 7, idx = wg >> 3;
  const int br = xcd >> 2, hp = (xcd & 3) * 4 + (idx & 3), b = idx >> 2;

  if (blockIdx.x == 0 && tid < 64) cnt[tid * 32] = 0u;
  DmaOff D;
#pragma unroll
  for (int j = 0; j < 6; ++j) {
    const int p = (w * 6 + j) * 64 + l;
    const int row = p / 24, chp = p % 24;
    D.k[j] = row * 384 + ((chp & ~7) | ((chp & 7) ^ ((row >> 1) & 7))) * 16;
    D.v[j] = row * 384 + ((chp & ~7) | ((chp & 7) ^ (((row >> 1) & 3) << 1))) * 16;
    D.w[j] = row * 12288 + ((chp & ~7) | ((chp & 7) ^ ((((row >> 1) & 1) << 1) | (((row >> 3) & 1) << 2)))) * 16;
    const int rx = p >> 3, cx = p & 7;
    D.x[j] = rx * 1280 + (cx ^ ((rx >> 1) & 7)) * 16;
  }
  const int kb0 = r * 384 + ((g) ^ (r >> 1)) * 16;
  const int kb1 = r * 384 + ((4 + g) ^ (r >> 1)) * 16;
  int vbs[4], wbs[4];
  {
    const int fv = ((2 * g + (q4 >> 1)) & 3) << 1;
    const int fw = ((q4 >> 1) << 1) | ((g & 1) << 2);
#pragma unroll
    for (int c = 0; c < 4; ++c) {
      vbs[c] = (4 * g + q4) * 384 + (((2 * c) ^ fv) + (p4 >> 1)) * 16 + 8 * (p4 & 1);
      wbs[c] = (8 * g + q4) * 384 + (((2 * c) ^ fw) + (p4 >> 1)) * 16 + 8 * (p4 & 1);
    }
  }
  const int xb0 = (48 * w + r) * 128 + ((g) ^ (r >> 1)) * 16;
  const int xb1 = (48 * w + r) * 128 + ((4 + g) ^ (r >> 1)) * 16;

  const unsigned xoff = (unsigned)b * (SQ * DM * 2);
  const unsigned woff = (unsigned)hp * (2 * DH * 2);
  const unsigned kvoff = (unsigned)(b * NH + 2 * hp) * (SK * DH * 2);

  const __amdgpu_buffer_rsrc_t rX = __builtin_amdgcn_make_buffer_rsrc((void*)(br ? xB : xA), 0, NB * SQ * DM * 2, 0x00020000);
  const __amdgpu_buffer_rsrc_t rW = __builtin_amdgcn_make_buffer_rsrc((void*)(br ? wqB : wqA), 0, DM * HD * 2, 0x00020000);
  const __amdgpu_buffer_rsrc_t rK = __builtin_amdgcn_make_buffer_rsrc((void*)(br ? kB : kA), 0, NB * NH * SK * DH * 2, 0x00020000);
  const __amdgpu_buffer_rsrc_t rV = __builtin_amdgcn_make_buffer_rsrc((void*)(br ? vB : vA), 0, NB * NH * SK * DH * 2, 0x00020000);

#define JOB_X(i, slot) mkjob(rX, D.x, xoff + (i) * 128u, smem, slot, w)
#define JOB_W(hh, i, slot) mkjob(rW, D.w, woff + (hh) * 384u + (i) * (64u * 12288u), smem, slot, w)
#define JOB_K(t, slot) mkjob(rK, D.k, kvoff + (t) * 24576u, smem, slot, w)
#define JOB_V(t, slot) mkjob(rV, D.v, kvoff + (t) * 24576u, smem, slot, w)
#define ISSUE(job) do { const DmaJob j_ = (job); _Pragma("unroll") for (int q_ = 0; q_ < 6; ++q_) dma_piece(j_, q_); } while (0)
  const DmaJob DUMMY = JOB_X(0, 0);

  ISSUE(JOB_X(0, 0)); ISSUE(JOB_W(0, 0, 1)); ISSUE(JOB_X(1, 2)); ISSUE(JOB_W(0, 1, 3)); ISSUE(JOB_X(2, 4)); ISSUE(JOB_W(0, 2, 5));

  const float QSCALE = 0.07216878364870322f * 1.4426950408889634f;
  constexpr std::true_type T_{}; constexpr std::false_type F_{};
  int ax = 0, aw = 1, bx = 2, bw = 3, cx = 4, cw = 5;

  for (int unit = 0; unit < 2; ++unit) {
    f32x4 acc[12][3];
    bf16x8 qf[3][6];
#pragma unroll
    for (int c = 0; c < 12; ++c)
#pragma unroll
      for (int qt = 0; qt < 3; ++qt) { acc[c][qt] = f32x4{0.f, 0.f, 0.f, 0.f}; asm volatile("" : "+a"(acc[c][qt])); }
    {
      bf16x8 xf[2][3];
      s16x4 wlo[4], whi[4];
      auto waddr = [&](const char* pw, int i) { const int ks1 = i / 12, c1 = i % 12; return pw + wbs[c1 & 3] + (32 * ks1) * 384 + (c1 >> 2) * 128; };
      auto qphase = [&](int sx, int sw, int sxn, int swn, auto nj_, const DmaJob& j0, const DmaJob& j1, auto hasNext_) {
        constexpr bool hasNext = decltype(hasNext_)::value;
        constexpr int nj = decltype(nj_)::value;
        const char* px = smem + sx * SLOT; const char* pw = smem + sw * SLOT;
        const char* pxn = smem + sxn * SLOT; const char* pwn = smem + swn * SLOT;
#pragma unroll
        for (int i = 0; i < 24; ++i) {
          const int ks = i / 12, c = i % 12;
          const bf16x8 wf = cat8(wlo[i & 3], whi[i & 3]);
          if constexpr (hasNext) { if (i == 21) { PHASE_SYNC(12); } }
          const bool ld = (i + 3 < 24) || hasNext;
          const char* an = (i + 3 < 24) ? waddr(pw, i + 3) : waddr(pwn, i + 3 - 24);
          mfma_a(acc[c][0], wf, xf[ks][0]);
          if (ld) wlo[(i + 3) & 3] = lds_tr(an);
          SB();
          mfma_a(acc[c][1], wf, xf[ks][1]);
          if (ld) whi[(i + 3) & 3] = lds_tr(an + 4 * 384);
          SB();
          mfma_a(acc[c][2], wf, xf[ks][2]);
          if (i < 3) xf[1][i] = lds_b128(px + xb1 + i * 2048);
          else if (i >= 21) { if constexpr (hasNext) xf[0][i - 21] = lds_b128(pxn + xb0 + (i - 21) * 2048); }
          else if (i % 3 != 2) { const int q = ((i - 3) / 3) * 2 + (i % 3); if (q < 6) { if constexpr (nj > 0) dma_piece(j0, q); } else { if constexpr (nj > 1) dma_piece(j1, q - 6); } }
          SB();
        }
      };
      constexpr std::integral_constant<int, 1> J1{}; constexpr std::integral_constant<int, 2> J2{};
      if (unit == 0) { PHASE_SYNC(24); } else { PHASE_SYNC(36); }
#pragma unroll
      for (int i = 0; i < 3; ++i) xf[0][i] = lds_b128(smem + ax * SLOT + xb0 + i * 2048);
#pragma unroll
      for (int i = 0; i < 3; ++i) { const char* a = waddr(smem + aw * SLOT, i); wlo[i] = lds_tr(a); whi[i] = lds_tr(a + 4 * 384); }
      qphase(ax, aw, bx, bw, J1, JOB_W(unit, 2, cw), DUMMY, T_);
      for (int i = 1; i < 8; ++i) {
        qphase(bx, bw, cx, cw, J2, JOB_X(i + 2, ax), JOB_W(unit, i + 2, aw), T_);
        { const int tx = ax, tw = aw; ax = bx; aw = bw; bx = cx; bw = cw; cx = tx; cw = tw; }
      }
      qphase(bx, bw, cx, cw, J2, JOB_K(8 * unit, ax), JOB_K(8 * unit + 1, aw), T_);
      qphase(cx, cw, cx, cw, J2, JOB_K(8 * unit + 2, bx), JOB_V(8 * unit, bw), F_);

      MFMA_DRAIN();
#pragma unroll
      for (int qt = 0; qt < 3; ++qt)
#pragma unroll
        for (int s = 0; s < 6; ++s) {
          asm volatile("" : "+a"(acc[2 * s][qt]), "+a"(acc[2 * s + 1][qt]));
          const f32x4 A = acc[2 * s][qt] * QSCALE, B = acc[2 * s + 1][qt] * QSCALE;
          u32x4 o = pair_fix(cvtpk(A[0], A[1]), cvtpk(A[2], A[3]), cvtpk(B[0], B[1]), cvtpk(B[2], B[3]));
          qf[qt][s] = __builtin_bit_cast(bf16x8, o);
          asm volatile("" : "+a"(qf[qt][s]));
          if (s % 3 == 2) SB();
        }
    }
#pragma unroll
    for (int c = 0; c < 12; ++c)
#pragma unroll
      for (int qt = 0; qt < 3; ++qt) { acc[c][qt] = f32x4{0.f, 0.f, 0.f, 0.f}; asm volatile("" : "+a"(acc[c][qt])); }

    float mref[3], lsum[3] = {0.f, 0.f, 0.f};
    f32x4 negm[3] = {f32x4{0.f, 0.f, 0.f, 0.f}, f32x4{0.f, 0.f, 0.f, 0.f}, f32x4{0.f, 0.f, 0.f, 0.f}};
    asm volatile("s_nop 1" : "+v"(negm[0]), "+v"(negm[1]), "+v"(negm[2]));
    f32x4 Sc[2][3];
    bf16x8 Pp[3];
    bf16x8 kf[3];
    s16x4 vlo[3], vhi[3];

    auto sm = [&](const f32x4 (&S)[2][3], bf16x8 (&P)[3], float (&ps)[3]) {
#pragma unroll
      for (int qt = 0; qt < 3; ++qt) {
        float e[8];
#pragma unroll
        for (int i = 0; i < 8; ++i) e[i] = __builtin_amdgcn_exp2f(S[i >> 2][qt][i & 3]);
        ps[qt] = ((e[0] + e[1]) + (e[2] + e[3])) + ((e[4] + e[5]) + (e[6] + e[7]));
        u32x4 o = {cvtpk(e[0], e[1]), cvtpk(e[2], e[3]), cvtpk(e[4], e[5]), cvtpk(e[6], e[7])};
        P[qt] = __builtin_bit_cast(bf16x8, o);
      }
    };
    auto rowmax = [&](const f32x4 (&S)[2][3], int qt) {
      float mx = fmaxf(fmaxf(fmaxf(S[0][qt][0], S[0][qt][1]), fmaxf(S[0][qt][2], S[0][qt][3])),
                       fmaxf(fmaxf(S[1][qt][0], S[1][qt][1]), fmaxf(S[1][qt][2], S[1][qt][3])));
      mx = fmaxf(mx, __shfl_xor(mx, 16));
      mx = fmaxf(mx, __shfl_xor(mx, 32));
      return mx;
    };
    auto kaddr = [&](const char* pk, int i) { const int s1 = i >> 1, k21 = i & 1; return pk + ((s1 & 1) ? kb1 : kb0) + k21 * 6144 + (s1 >> 1) * 128; };
    auto vaddr = [&](const char* pv, int c1) { return pv + vbs[c1 & 3] + (c1 >> 2) * 128; };
    auto kbase = [&](int slot, int hb) { return (const char*)smem + slot * SLOT + hb * (2 * 6144); };
    auto vbase = [&](int slot, int ks) { return (const char*)smem + slot * SLOT + (32 * ks) * 384; };
    auto step = [&](auto doQK_, const char* pk, auto doPV_, const char* pvv, auto doSM_, auto hasJob_, const DmaJob& jb,
                    auto nQK_, const char* pkn, auto nPV_, const char* pvn, auto hasSync_) {
      constexpr bool hasJob = decltype(hasJob_)::value;
      constexpr bool doQK = decltype(doQK_)::value, doPV = decltype(doPV_)::value, doSM = decltype(doSM_)::value;
      constexpr bool nQK = decltype(nQK_)::value, nPV = decltype(nPV_)::value, hasSync = decltype(hasSync_)::value;
      f32x4 Sn[2][3];
      u32x4 Pn[3];
      float ps[3] = {0.f, 0.f, 0.f};
      float ea = 0.f, eb = 0.f;
#pragma unroll
      for (int i = 0; i < 12; ++i) {
        const int s = i >> 1, k2 = i & 1, ri = i % 3, rn = (i + 2) % 3;
        if constexpr (hasSync) { if (i == 10) { PHASE_SYNC(12); } }
        const bool in = (i + 2 < 12);
        const bool ldK = in ? doQK : nQK, ldV = in ? doPV : nPV;
        const char* ka = in ? kaddr(pk, i + 2) : kaddr(pkn, i + 2 - 12);
        const char* va = in ? vaddr(pvv, i + 2) : vaddr(pvn, i + 2 - 12);
        const bf16x8 kc = kf[ri];
        const bf16x8 vc = cat8(vlo[ri], vhi[ri]);
        const int pq = (i - 1) >> 2, pj = (i - 1) & 3;
        const int eq = i >> 2, e0 = (i & 3) * 2;
        if constexpr (doQK) { if (s == 0) mfma_v0(Sn[k2][0], kc, qf[0][s], negm[0]); else mfma_v(Sn[k2][0], kc, qf[0][s]); }
        if (ldV) vlo[rn] = lds_tr(va);
        if constexpr (doSM) { if (i > 0) { unsigned pk2 = cvtpk(ea, eb); asm volatile("" : "+v"(pk2)); Pn[pq][pj] = pk2; } }
        SB();
        if constexpr (doQK) { if (s == 0) mfma_v0(Sn[k2][1], kc, qf[1][s], negm[1]); else mfma_v(Sn[k2][1], kc, qf[1][s]); }
        if (ldV) vhi[rn] = lds_tr(va + 16 * 384);
        if constexpr (doSM) { if (i > 0) { if (pj == 0) ps[pq] = fadd_s(ea, eb); else ps[pq] = fadd_s(ps[pq], ea); } }
        SB();
        if constexpr (doQK) { if (s == 0) mfma_v0(Sn[k2][2], kc, qf[2][s], negm[2]); else mfma_v(Sn[k2][2], kc, qf[2][s]); }
        if (ldK) kf[rn] = lds_b128(ka);
        if constexpr (doSM) { if (i > 0) { if (pj != 0) ps[pq] = fadd_s(ps[pq], eb); } }
        SB();
        if constexpr (doPV) mfma_a(acc[i][0], vc, Pp[0]);
        if constexpr (doSM) { ea = __builtin_amdgcn_exp2f(Sc[e0 >> 2][eq][e0 & 3]); asm volatile("" : "+v"(ea)); }
        SB();
        if constexpr (doPV) mfma_a(acc[i][1], vc, Pp[1]);
        if constexpr (doSM) { eb = __builtin_amdgcn_exp2f(Sc[e0 >> 2][eq][(e0 & 3) + 1]); asm volatile("" : "+v"(eb)); }
        SB();
        if constexpr (doPV) mfma_a(acc[i][2], vc, Pp[2]);
        if constexpr (hasJob) { if (i < 10) { if ((i & 1) == 0) dma_piece(jb, i >> 1); else if (i == 9) dma_piece(jb, 5); } }
        if constexpr (doQK) { if (i == 2) asm volatile("" :: "v"(negm[0]), "v"(negm[1]), "v"(negm[2])); }
        SB();
      }
      if constexpr (doSM) {
        Pn[2][3] = cvtpk(ea, eb);
        ps[2] = fadd_s(ps[2], ea); ps[2] = fadd_s(ps[2], eb);
        const bool bad = !(ps[0] <= 1.0e12f) || !(ps[1] <= 1.0e12f) || !(ps[2] <= 1.0e12f);
        bf16x8 Pb[3];
#pragma unroll
        for (int qt = 0; qt < 3; ++qt) Pb[qt] = __builtin_bit_cast(bf16x8, Pn[qt]);
        if (__builtin_expect(__any(bad), 0)) {
          MFMA_DRAIN();
          if constexpr (doQK) asm volatile("" : "+v"(Sn[0][0]), "+v"(Sn[0][1]), "+v"(Sn[0][2]), "+v"(Sn[1][0]), "+v"(Sn[1][1]), "+v"(Sn[1][2]));
#pragma unroll
          for (int qt = 0; qt < 3; ++qt) {
            const float d = fmaxf(rowmax(Sc, qt), 0.f);
            const float f = __builtin_amdgcn_exp2f(-d);
            mref[qt] += d;
            negm[qt] = f32x4{-mref[qt], -mref[qt], -mref[qt], -mref[qt]};
            asm volatile("s_nop 1" : "+v"(negm[qt]));
            lsum[qt] *= f;
#pragma unroll
            for (int c = 0; c < 12; ++c) { asm volatile("" : "+a"(acc[c][qt])); acc[c][qt] *= f; asm volatile("" : "+a"(acc[c][qt])); SB(); }
#pragma unroll
            for (int k2 = 0; k2 < 2; ++k2) { Sc[k2][qt] -= d; if constexpr (doQK) Sn[k2][qt] -= d; }
          }
          sm(Sc, Pb, ps);
        }
#pragma unroll
        for (int qt = 0; qt < 3; ++qt) { lsum[qt] += ps[qt]; Pp[qt] = Pb[qt]; asm volatile("s_nop 0" : "+v"(Pp[qt])); }
      }
      if constexpr (doQK) {
#pragma unroll
        for (int qt = 0; qt < 3; ++qt) { Sc[0][qt] = Sn[0][qt]; Sc[1][qt] = Sn[1][qt]; }
      }
    };

    const int G0 = unit * 8;
    int kc = ax, kn = aw, kp = bx;
    int vn = bw, vp = cx, vc = cw;
#define ROTK() do { const int x_ = kp; kp = kc; kc = kn; kn = x_; } while (0)
#define ROTV() do { const int x_ = vp; vp = vc; vc = vn; vn = x_; } while (0)
    PHASE_SYNC(18);
    {
      kf[0] = lds_b128(kaddr(kbase(kc, 0), 0)); kf[1] = lds_b128(kaddr(kbase(kc, 0), 1));
      step(T_, kbase(kc, 0), F_, smem, F_, T_, JOB_V(G0 + 1, vp), T_, kbase(kc, 1), F_, smem, F_);
      asm volatile("s_nop 7\n\ts_nop 7" : "+v"(Sc[0][0]), "+v"(Sc[0][1]), "+v"(Sc[0][2]), "+v"(Sc[1][0]), "+v"(Sc[1][1]), "+v"(Sc[1][2]));
#pragma unroll
      for (int qt = 0; qt < 3; ++qt) {
        const float mx = rowmax(Sc, qt);
        mref[qt] = mx;
        negm[qt] = f32x4{-mx, -mx, -mx, -mx};
        asm volatile("s_nop 1" : "+v"(negm[qt]));
        Sc[0][qt] -= mx; Sc[1][qt] -= mx;
      }
      step(T_, kbase(kc, 1), F_, smem, T_, T_, JOB_V(G0 + 2, vc), T_, kbase(kn, 0), T_, vbase(vn, 0), T_);
      ROTK(); ROTV();
      step(T_, kbase(kc, 0), T_, vbase(vc, 0), T_, T_, JOB_K(G0 + 3, kp), T_, kbase(kc, 1), T_, vbase(vc, 1), F_);
      step(T_, kbase(kc, 1), T_, vbase(vc, 1), T_, F_, DUMMY, T_, kbase(kn, 0), T_, vbase(vn, 0), T_);
      ROTK(); ROTV();
    }
    for (int t = 2; t < 6; ++t) {
      step(T_, kbase(kc, 0), T_, vbase(vc, 0), T_, T_, JOB_K(G0 + t + 2, kp), T_, kbase(kc, 1), T_, vbase(vc, 1), F_);
      step(T_, kbase(kc, 1), T_, vbase(vc, 1), T_, T_, JOB_V(G0 + t + 1, vp), T_, kbase(kn, 0), T_, vbase(vn, 0), T_);
      ROTK(); ROTV();
    }
    {
      ax = kp;
      step(T_, kbase(kc, 0), T_, vbase(vc, 0), T_, T_, JOB_X(0, ax), T_, kbase(kc, 1), T_, vbase(vc, 1), F_);
      step(T_, kbase(kc, 1), T_, vbase(vc, 1), T_, T_, JOB_V(G0 + 7, vp), T_, kbase(kn, 0), T_, vbase(vn, 0), T_);
      ROTK(); ROTV();
      aw = kp; bx = vp;
      step(T_, kbase(kc, 0), T_, vbase(vc, 0), T_, T_, JOB_W(1, 0, aw), T_, kbase(kc, 1), T_, vbase(vc, 1), F_);
      step(T_, kbase(kc, 1), T_, vbase(vc, 1), T_, T_, JOB_X(1, bx), F_, smem, T_, vbase(vn, 0), T_);
      ROTK(); ROTV();
      bw = kp; cx = vp; cw = vc;
      step(F_, smem, T_, vbase(vc, 0), T_, T_, JOB_W(1, 1, bw), F_, smem, T_, vbase(vc, 1), F_);
      step(F_, smem, T_, vbase(vc, 1), F_, T_, JOB_X(2, cx), F_, smem, F_, smem, F_);
    }

    MFMA_DRAIN();
    {
      u16* orow = Og + (size_t)br * MROWS * HD + ((size_t)(3 * (2 * hp + unit)) * MROWS + (b * SQ + 48 * w + r)) * 64 + 8 * g;
#pragma unroll
      for (int qt = 0; qt < 3; ++qt) {
        float lt = lsum[qt];
        lt += __shfl_xor(lt, 16);
        lt += __shfl_xor(lt, 32);
        const float inv = 1.0f / lt;
#pragma unroll
        for (int s = 0; s < 6; ++s) {
          asm volatile("" : "+a"(acc[2 * s][qt]), "+a"(acc[2 * s + 1][qt]));
          const f32x4 A = acc[2 * s][qt] * inv, B = acc[2 * s + 1][qt] * inv;
          u32x4 o = pair_fix(cvtpk(A[0], A[1]), cvtpk(A[2], A[3]), cvtpk(B[0], B[1]), cvtpk(B[2], B[3]));
          *reinterpret_cast<u32x4*>(orow + (size_t)(s >> 1) * MROWS * 64 + qt * 16 * 64 + 32 * (s & 1)) = o;
          if (s % 3 == 2) SB();
        }
      }
    }
    STAMP();
  }
  asm volatile("s_waitcnt vmcnt(0)" ::: "memory");
}

constexpr int K2_STAGE = 24576 + 20480;
constexpr int K2_LDS = 3 * K2_STAGE;
#define K2_PF 5
constexpr size_t WS_O_BYTES = (size_t)2 * MROWS * HD * 2;
constexpr size_t WS_PART_OFF = WS_O_BYTES;
constexpr size_t WS_PART_BYTES = (size_t)64 * 4 * 8 * 15360;
constexpr size_t WS_CNT_OFF = WS_PART_OFF + WS_PART_BYTES;

__global__ __launch_bounds__(576, 2) void k2_oproj(const u16* __restrict__ Og, const u16* __restrict__ woA, const u16* __restrict__ woB,
                                                   u16* __restrict__ out, float* __restrict__ part, unsigned* __restrict__ cnt) {
  extern __shared__ __attribute__((aligned(16))) char smem[];
  const int tid = threadIdx.x, l = tid & 63;
  const int w = __builtin_amdgcn_readfirstlane(tid >> 6);
  const int r = l & 15, g = l >> 4, q4 = (l & 15) >> 2, p4 = l & 3;
  const int mw = w >> 1, nw = w & 1;
  const int wg = blockIdx.x, xcd = wg & 7, idx = wg >> 3;
  const int br = xcd >> 2, sk = xcd & 3, mt = idx >> 2, nt = idx & 3;
  const int tl = br * 32 + idx;
  const u16* wo = br ? woB : woA;
  const __amdgpu_buffer_rsrc_t rO = __builtin_amdgcn_make_buffer_rsrc((void*)(Og + (size_t)br * MROWS * HD), 0, MROWS * HD * 2, 0x00020000);
  const __amdgpu_buffer_rsrc_t rW = __builtin_amdgcn_make_buffer_rsrc((void*)wo, 0, HD * DM * 2, 0x00020000);
  const unsigned ooff = ((unsigned)(sk * 24) * MROWS + (unsigned)(mt * 192)) * 128u;
  const unsigned woff = (unsigned)(sk * 1536) * (DM * 2) + (unsigned)nt * 320;
  if (w == 8) {
    const unsigned to = (l < 48) ? (unsigned)(nt * 48 + l) * 128u : 0x7fffffffu;
    const unsigned tw = (l < 24) ? (unsigned)(mt * 8 + l / 3) * (DM * 2) + (unsigned)(l % 3) * 128u : 0x7fffffffu;
    unsigned junk = 0;
    auto tch = [&](int st) {
      junk ^= __builtin_amdgcn_raw_buffer_load_b32(rO, to, ooff + st * (MROWS * 128u), 0);
      junk ^= __builtin_amdgcn_raw_buffer_load_b32(rW, tw, (woff & ~127u) + st * (64u * DM * 2), 0);
    };
#pragma unroll
    for (int st = 2; st < 2 + K2_PF; ++st) tch(st);
    __builtin_amdgcn_s_barrier();
    for (int st = 0; st < 24; ++st) {
      if (st + 2 + K2_PF < 24) tch(st + 2 + K2_PF);
      __builtin_amdgcn_s_barrier();
    }
    if (junk == 0x9e3779b9u) cnt[tl * 32 + 1] = junk;
    return;
  }
  unsigned vo[3], vw[3], lo_[3], lw_[3];
#pragma unroll
  for (int j = 0; j < 3; ++j) {
    const int pi = w + 8 * j;
    const int p = pi * 64 + l, rx = p >> 3, cx = p & 7;
    vo[j] = rx * 128 + (cx ^ ((rx >> 1) & 7)) * 16;
    lo_[j] = pi * 1024;
    int wi = w + 8 * j; if (wi >= 20) wi -= 4;
    const int pw = wi * 64 + l, row = pw / 20, ch = pw % 20;
    vw[j] = row * (DM * 2) + (ch ^ (((row >> 3) & 1) << 1)) * 16;
    lw_[j] = 24576 + wi * 1024;
  }
  const unsigned lbase = (unsigned)(size_t)smem;
  auto issue1 = [&](int st, int buf, int j) {
    const unsigned lb = lbase + buf * K2_STAGE;
    if (j & 1) asm volatile("s_mov_b32 m0, %0\n\ts_nop 0\n\tbuffer_load_dwordx4 %1, %2, %3 offen lds" :: "s"(lb + lw_[j >> 1]), "v"(vw[j >> 1]), "s"(rW), "s"(woff + st * (64u * DM * 2)) : "memory");
    else       asm volatile("s_mov_b32 m0, %0\n\ts_nop 0\n\tbuffer_load_dwordx4 %1, %2, %3 offen lds" :: "s"(lb + lo_[j >> 1]), "v"(vo[j >> 1]), "s"(rO), "s"(ooff + st * (MROWS * 128u)) : "memory");
  };
  const int ob0 = (48 * mw + r) * 128 + ((g) ^ (r >> 1)) * 16;
  const int ob1 = (48 * mw + r) * 128 + ((4 + g) ^ (r >> 1)) * 16;
  int wb[5];
#pragma unroll
  for (int c = 0; c < 5; ++c) wb[c] = 24576 + (8 * g + q4) * 320 + (p4 >> 1) * 16 + 8 * (p4 & 1) + (((5 * nw + c) ^ (g & 1)) * 32);
#define ST2() do {} while (0)
  f32x4 acc[5][3] = {};
  bf16x8 of0[3], wf0[5], of1[3], wf1[5];
  auto ldfr = [&](bf16x8 (&of)[3], bf16x8 (&wf)[5], const char* pb, int ks) {
#pragma unroll
    for (int m = 0; m < 3; ++m) of[m] = lds_b128(pb + (ks ? ob1 : ob0) + m * 2048);
#pragma unroll
    for (int c = 0; c < 5; ++c) { const char* a0 = pb + wb[c] + (32 * ks) * 320; wf[c] = cat8(lds_tr(a0), lds_tr(a0 + 4 * 320)); }
  };
  ST2();
#pragma unroll
  for (int j = 0; j < 6; ++j) issue1(0, 0, j);
#pragma unroll
  for (int j = 0; j < 6; ++j) issue1(1, 1, j);
  asm volatile("s_waitcnt vmcnt(6)" ::: "memory");
  __builtin_amdgcn_s_barrier();
  asm volatile("" ::: "memory");
  ldfr(of0, wf0, smem, 0);
  int buf = 0;
  for (int st = 0; st < 24; ++st) {
    const char* pb = smem + buf * K2_STAGE;
    const int bn = (buf == 2) ? 0 : buf + 1, bp = (buf == 0) ? 2 : buf - 1;
    ST2();
    ldfr(of1, wf1, pb, 1);
    SB();
#pragma unroll
    for (int c = 0; c < 5; ++c) {
#pragma unroll
      for (int m = 0; m < 3; ++m) acc[c][m] = mfma16(wf0[c], of0[m], acc[c][m]);
      if (st + 2 < 24) { issue1(st + 2, bp, c); if (c == 4) issue1(st + 2, bp, 5); }
      SB();
    }
    ST2();
    if (st < 22) { asm volatile("s_waitcnt vmcnt(6) lgkmcnt(0)" ::: "memory"); } else { asm volatile("s_waitcnt vmcnt(0) lgkmcnt(0)" ::: "memory"); }
    __builtin_amdgcn_s_barrier();
    asm volatile("" ::: "memory");
    ST2();
    if (st + 1 < 24) ldfr(of0, wf0, smem + bn * K2_STAGE, 0);
    SB();
#pragma unroll
    for (int c = 0; c < 5; ++c)
#pragma unroll
      for (int m = 0; m < 3; ++m) acc[c][m] = mfma16(wf1[c], of1[m], acc[c][m]);
    buf = bn;
  }
  ST2();
  float* slab = part + ((size_t)(tl * 4) * 8) * 3840;
  const __amdgpu_buffer_rsrc_t rP = __builtin_amdgcn_make_buffer_rsrc((void*)slab, 0, 4 * 8 * 15360, 0x00020000);
#pragma unroll
  for (int c = 0; c < 5; ++c)
#pragma unroll
    for (int m = 0; m < 3; ++m) {
      const int owner = (c == 4) ? 3 : m;
      if (owner != sk) __builtin_amdgcn_raw_buffer_store_b128(__builtin_bit_cast(u32x4, acc[c][m]), rP, l * 16, (unsigned)((sk * 8 + w) * 15360 + (c * 3 + m) * 1024), 16);
    }
  asm volatile("s_waitcnt vmcnt(0)" ::: "memory");
  __syncthreads();
  if (tid == 0) __hip_atomic_fetch_add(cnt + tl * 32, 1u, __ATOMIC_RELAXED, __HIP_MEMORY_SCOPE_AGENT);
  ST2();
  if (w == 0) {
    unsigned spins = 0;
    while (__hip_atomic_load(cnt + tl * 32, __ATOMIC_RELAXED, __HIP_MEMORY_SCOPE_AGENT) < 4u) { __builtin_amdgcn_s_sleep(1); if (++spins > (1u << 24)) break; }
  }
  __syncthreads();
  ST2();
  u16* orow = out + (size_t)(mt * 192 + 48 * mw + r) * (2 * DM) + br * DM + nt * 160 + 80 * nw;
  auto fin = [&](auto J_) {
    constexpr int J = decltype(J_)::value;
    constexpr int NT = (J == 3) ? 3 : 4;
    u32x4 v[3][NT];
#pragma unroll
    for (int s2 = 0; s2 < 3; ++s2) {
      const int src = s2 + (s2 >= J ? 1 : 0);
#pragma unroll
      for (int i = 0; i < NT; ++i) {
        const int ti = (J == 3) ? (12 + i) : (i * 3 + J);
        v[s2][i] = __builtin_amdgcn_raw_buffer_load_b128(rP, l * 16, (unsigned)((src * 8 + w) * 15360 + ti * 1024), 16);
      }
    }
    f32x4 sum[NT];
#pragma unroll
    for (int i = 0; i < NT; ++i) {
      const f32x4 own = (J == 3) ? acc[4][i] : acc[i][J];
      f32x4 t[4];
#pragma unroll
      for (int s4 = 0; s4 < 4; ++s4) t[s4] = (s4 == J) ? own : __builtin_bit_cast(f32x4, v[s4 - (s4 > J ? 1 : 0)][i]);
      sum[i] = (t[0] + t[1]) + (t[2] + t[3]);
    }
    if constexpr (J == 3) {
#pragma unroll
      for (int m = 0; m < 3; ++m) {
        uint2 e2 = make_uint2(cvtpk(sum[m][0], sum[m][1]), cvtpk(sum[m][2], sum[m][3]));
        *reinterpret_cast<uint2*>(orow + (size_t)m * 16 * (2 * DM) + 64 + 4 * g) = e2;
      }
    } else {
#pragma unroll
      for (int c = 0; c < 4; c += 2) {
        const f32x4 A = sum[c], B = sum[c + 1];
        u32x4 o = pair_fix(cvtpk(A[0], A[1]), cvtpk(A[2], A[3]), cvtpk(B[0], B[1]), cvtpk(B[2], B[3]));
        *reinterpret_cast<u32x4*>(orow + (size_t)J * 16 * (2 * DM) + 16 * c + 8 * g) = o;
      }
    }
  };
  if (sk == 0) fin(std::integral_constant<int, 0>{});
  else if (sk == 1) fin(std::integral_constant<int, 1>{});
  else if (sk == 2) fin(std::integral_constant<int, 2>{});
  else fin(std::integral_constant<int, 3>{});
  ST2();
}

extern "C" void kernel_launch(void* const* d_in, const int* in_sizes, int n_in,
                              void* d_out, int out_size, void* d_ws, size_t ws_size,
                              hipStream_t stream) {
  const u16* xA = (const u16*)d_in[0]; const u16* kA = (const u16*)d_in[1]; const u16* vA = (const u16*)d_in[2];
  const u16* wqA = (const u16*)d_in[3]; const u16* woA = (const u16*)d_in[4];
  const u16* xB = (const u16*)d_in[5]; const u16* kB = (const u16*)d_in[6]; const u16* vB = (const u16*)d_in[7];
  const u16* wqB = (const u16*)d_in[8]; const u16* woB = (const u16*)d_in[9];
  u16* Og = (u16*)d_ws;
  float* part = (float*)((char*)d_ws + WS_PART_OFF);
  unsigned* cnt = (unsigned*)((char*)d_ws + WS_CNT_OFF);
  (void)hipFuncSetAttribute((const void*)k1_attn, hipFuncAttributeMaxDynamicSharedMemorySize, K1_LDS);
  (void)hipFuncSetAttribute((const void*)k2_oproj, hipFuncAttributeMaxDynamicSharedMemorySize, K2_LDS);
  hipLaunchKernelGGL(k1_attn, dim3(256), dim3(256), K1_LDS, stream, xA, kA, vA, wqA, xB, kB, vB, wqB, Og, cnt);
  hipLaunchKernelGGL(k2_oproj, dim3(256), dim3(576), K2_LDS, stream, Og, woA, woB, (u16*)d_out, part, cnt);
}
```

```cpp
#include <hip/hip_runtime.h>
#include <hip/hip_bf16.h>
#include <stdint.h>
#include <type_traits>

#define DI __device__ __forceinline__
typedef unsigned short u16;
typedef __attribute__((ext_vector_type(8))) __bf16 bf16x8;
typedef __attribute__((ext_vector_type(2))) __bf16 bf16x2;
typedef __attribute__((ext_vector_type(4))) short s16x4;
typedef __attribute__((ext_vector_type(8))) short s16x8;
typedef __attribute__((ext_vector_type(4))) float f32x4;
typedef __attribute__((ext_vector_type(2))) float f32x2;
typedef __attribute__((ext_vector_type(4))) unsigned u32x4;
typedef __attribute__((address_space(3))) void lds_void;
typedef __attribute__((address_space(3))) s16x4 lds_s16x4;

constexpr int NB = 8, SQ = 192, DM = 640, NH = 32, SK = 512, DH = 192;
constexpr int HD = NH * DH;
constexpr int MROWS = NB * SQ;
constexpr int SLOT = 24576;
constexpr int K1_LDS = 160 * 1024;

DI unsigned cvtpk(float lo, float hi) {
  f32x2 v = {lo, hi};
  bf16x2 b = __builtin_convertvector(v, bf16x2);
  return __builtin_bit_cast(unsigned, b);
}
DI f32x4 mfma16(bf16x8 a, bf16x8 b, f32x4 c) { return __builtin_amdgcn_mfma_f32_16x16x32_bf16(a, b, c, 0, 0, 0); }
DI void mfma_a(f32x4& c, bf16x8 a, bf16x8 b) { asm volatile("v_mfma_f32_16x16x32_bf16 %0, %1, %2, %0" : "+a"(c) : "v"(a), "v"(b)); }
DI void mfma_v(f32x4& c, bf16x8 a, bf16x8 b) { asm volatile("v_mfma_f32_16x16x32_bf16 %0, %1, %2, %0" : "+v"(c) : "v"(a), "a"(b)); }
DI void mfma_vv(f32x4& c, bf16x8 a, bf16x8 b) { asm volatile("v_mfma_f32_16x16x32_bf16 %0, %1, %2, %0" : "+v"(c) : "v"(a), "v"(b)); }
DI void mfma_v0(f32x4& d, bf16x8 a, bf16x8 b, f32x4 c) { asm volatile("v_mfma_f32_16x16x32_bf16 %0, %1, %2, %3" : "=&v"(d) : "v"(a), "a"(b), "v"(c)); }
DI float fadd_s(float a, float b) { float r = a + b; asm("" : "+v"(r)); return r; }
#define MFMA_DRAIN() asm volatile("s_nop 7\n\ts_nop 7" ::: "memory")
DI bf16x8 lds_b128(const char* p) { return *reinterpret_cast<const bf16x8*>(p); }
DI bf16x8 lds_tr2(const char* p0, const char* p1) {
  s16x4 lo = __builtin_amdgcn_ds_read_tr16_b64_v4i16((lds_s16x4*)p0);
  s16x4 hi = __builtin_amdgcn_ds_read_tr16_b64_v4i16((lds_s16x4*)p1);
  s16x8 v = __builtin_shufflevector(lo, hi, 0, 1, 2, 3, 4, 5, 6, 7);
  return __builtin_bit_cast(bf16x8, v);
}
DI s16x4 lds_tr(const char* p) { return __builtin_amdgcn_ds_read_tr16_b64_v4i16((lds_s16x4*)p); }
DI bf16x8 cat8(s16x4 lo, s16x4 hi) { s16x8 v = __builtin_shufflevector(lo, hi, 0, 1, 2, 3, 4, 5, 6, 7); return __builtin_bit_cast(bf16x8, v); }
#define SB() __builtin_amdgcn_sched_barrier(0)
#define WAITV(n) asm volatile("s_waitcnt vmcnt(" #n ")" ::: "memory")
#define STAMP() do {} while (0)
#define PHASE_SYNC(n) do { STAMP(); asm volatile("s_waitcnt vmcnt(" #n ") lgkmcnt(0)" ::: "memory"); STAMP(); __builtin_amdgcn_s_barrier(); asm volatile("" ::: "memory"); STAMP(); } while (0)

DI u32x4 pair_fix(unsigned a0, unsigned a1, unsigned b0, unsigned b1) {
  auto r0 = __builtin_amdgcn_permlane32_swap(a0, b0, false, false);
  auto r1 = __builtin_amdgcn_permlane32_swap(a1, b1, false, false);
  auto s0 = __builtin_amdgcn_permlane16_swap(r0[0], r0[1], false, false);
  auto s1 = __builtin_amdgcn_permlane16_swap(r1[0], r1[1], false, false);
  u32x4 o = {s0[0], s1[0], s0[1], s1[1]};
  return o;
}

struct DmaOff { unsigned k[6], v[6], w[6], x[6]; };

DI void dma_tile(__amdgpu_buffer_rsrc_t rs, const unsigned (&vo)[6], unsigned soff, char* smem, int slot, int w) {
  const unsigned lb = (unsigned)(size_t)smem + (unsigned)(slot * SLOT + w * 6144);
#pragma unroll
  for (int j = 0; j < 6; ++j)
    asm volatile("s_mov_b32 m0, %0\n\ts_nop 0\n\tbuffer_load_dwordx4 %1, %2, %3 offen lds" :: "s"(lb + j * 1024u), "v"(vo[j]), "s"(rs), "s"(soff) : "memory");
}

struct DmaJob { __amdgpu_buffer_rsrc_t rs; unsigned soff, lb; unsigned vo[6]; int valid; };
DI DmaJob mkjob(__amdgpu_buffer_rsrc_t rs, const unsigned (&vo)[6], unsigned soff, char* smem, int slot, int w) {
  DmaJob j; j.rs = rs; j.soff = soff; j.lb = (unsigned)(size_t)smem + (unsigned)(slot * SLOT + w * 6144); j.valid = 1;
#pragma unroll
  for (int i = 0; i < 6; ++i) j.vo[i] = vo[i];
  return j;
}
struct DmaJobNT : DmaJob {};
DI DmaJobNT mkjob_nt(__amdgpu_buffer_rsrc_t rs, const unsigned (&vo)[6], unsigned soff, char* smem, int slot, int w) { DmaJobNT j; (DmaJob&)j = mkjob(rs, vo, soff, smem, slot, w); return j; }
DI void dma_piece(const DmaJobNT& jb, int j) {
  asm volatile("s_mov_b32 m0, %0\n\ts_nop 0\n\tbuffer_load_dwordx4 %1, %2, %3 offen nt lds" :: "s"(jb.lb + j * 1024u), "v"(jb.vo[j]), "s"(jb.rs), "s"(jb.soff) : "memory");
}
DI void fixjob(DmaJob& j) { j.soff = __builtin_amdgcn_readfirstlane(j.soff); j.lb = __builtin_amdgcn_readfirstlane(j.lb); }
DI void dma_piece(const DmaJob& jb, int j) {
  asm volatile("s_mov_b32 m0, %0\n\ts_nop 0\n\tbuffer_load_dwordx4 %1, %2, %3 offen lds" :: "s"(jb.lb + j * 1024u), "v"(jb.vo[j]), "s"(jb.rs), "s"(jb.soff) : "memory");
}

__global__ __launch_bounds__(256, 1) void k1_attn(const u16* __restrict__ xA, const u16* __restrict__ kA, const u16* __restrict__ vA, const u16* __restrict__ wqA,
                                                  const u16* __restrict__ xB, const u16* __restrict__ kB, const u16* __restrict__ vB, const u16* __restrict__ wqB,
                                                  u16* __restrict__ Og, unsigned* __restrict__ cnt) {
  extern __shared__ __attribute__((aligned(16))) char smem[];
  const int tid = threadIdx.x, l = tid & 63;
  const int w = __builtin_amdgcn_readfirstlane(tid >> 6);
  const int r = l & 15, g = l >> 4, q4 = (l & 15) >> 2, p4 = l & 3;
  const int wg = blockIdx.x, xcd = wg & 7, idx = wg >> 3;
  const int br = xcd >> 2, hp = (xcd & 3) * 4 + (idx & 3), b = idx >> 2;

  if (blockIdx.x == 0 && tid < 80) cnt[tid * 32] = 0u;
  DmaOff D;
#pragma unroll
  for (int j = 0; j < 6; ++j) {
    const int p = (w * 6 + j) * 64 + l;
    const int row = p / 24, chp = p % 24;
    D.k[j] = row * 384 + ((chp & ~7) | ((chp & 7) ^ ((row >> 1) & 7))) * 16;
    D.v[j] = row * 384 + ((chp & ~7) | ((chp & 7) ^ (((row >> 1) & 3) << 1))) * 16;
    D.w[j] = row * 12288 + ((chp & ~7) | ((chp & 7) ^ ((((row >> 1) & 1) << 1) | (((row >> 3) & 1) << 2)))) * 16;
    const int rx = p >> 3, cx = p & 7;
    D.x[j] = rx * 1280 + (cx ^ ((rx >> 1) & 7)) * 16;
  }
  const int kb0 = r * 384 + ((g) ^ (r >> 1)) * 16;
  const int kb1 = r * 384 + ((4 + g) ^ (r >> 1)) * 16;
  int vbs[4], wbs[4];
  {
    const int fv = ((2 * g + (q4 >> 1)) & 3) << 1;
    const int fw = ((q4 >> 1) << 1) | ((g & 1) << 2);
#pragma unroll
    for (int c = 0; c < 4; ++c) {
      vbs[c] = (4 * g + q4) * 384 + (((2 * c) ^ fv) + (p4 >> 1)) * 16 + 8 * (p4 & 1);
      wbs[c] = (8 * g + q4) * 384 + (((2 * c) ^ fw) + (p4 >> 1)) * 16 + 8 * (p4 & 1);
    }
  }
  const int xb0 = (48 * w + r) * 128 + ((g) ^ (r >> 1)) * 16;
  const int xb1 = (48 * w + r) * 128 + ((4 + g) ^ (r >> 1)) * 16;

  const unsigned xoff = (unsigned)b * (SQ * DM * 2);
  const unsigned woff = (unsigned)hp * (2 * DH * 2);
  const unsigned kvoff = (unsigned)(b * NH + 2 * hp) * (SK * DH * 2);

  const __amdgpu_buffer_rsrc_t rX = __builtin_amdgcn_make_buffer_rsrc((void*)(br ? xB : xA), 0, NB * SQ * DM * 2, 0x00020000);
  const __amdgpu_buffer_rsrc_t rW = __builtin_amdgcn_make_buffer_rsrc((void*)(br ? wqB : wqA), 0, DM * HD * 2, 0x00020000);
  const __amdgpu_buffer_rsrc_t rK = __builtin_amdgcn_make_buffer_rsrc((void*)(br ? kB : kA), 0, NB * NH * SK * DH * 2, 0x00020000);
  const __amdgpu_buffer_rsrc_t rV = __builtin_amdgcn_make_buffer_rsrc((void*)(br ? vB : vA), 0, NB * NH * SK * DH * 2, 0x00020000);

#define JOB_X(i, slot) mkjob(rX, D.x, xoff + (i) * 128u, smem, slot, w)
#define JOB_W(hh, i, slot) mkjob(rW, D.w, woff + (hh) * 384u + (i) * (64u * 12288u), smem, slot, w)
#define JOB_K(t, slot) mkjob_nt(rK, D.k, kvoff + (t) * 24576u, smem, slot, w)
#define JOB_V(t, slot) mkjob_nt(rV, D.v, kvoff + (t) * 24576u, smem, slot, w)
#define ISSUE(job) do { const DmaJob j_ = (job); _Pragma("unroll") for (int q_ = 0; q_ < 6; ++q_) dma_piece(j_, q_); } while (0)
  const DmaJob DUMMY = JOB_X(0, 0);

  ISSUE(JOB_X(0, 0)); ISSUE(JOB_W(0, 0, 1)); ISSUE(JOB_X(1, 2)); ISSUE(JOB_W(0, 1, 3)); ISSUE(JOB_X(2, 4)); ISSUE(JOB_W(0, 2, 5));

  const float QSCALE = 0.07216878364870322f * 1.4426950408889634f;
  constexpr std::true_type T_{}; constexpr std::false_type F_{};
  int ax = 0, aw = 1, bx = 2, bw = 3, cx = 4, cw = 5;

  for (int unit = 0; unit < 2; ++unit) {
    f32x4 acc[12][3];
    bf16x8 qf[3][6];
#pragma unroll
    for (int c = 0; c < 12; ++c)
#pragma unroll
      for (int qt = 0; qt < 3; ++qt) { acc[c][qt] = f32x4{0.f, 0.f, 0.f, 0.f}; asm volatile("" : "+a"(acc[c][qt])); }
    {
      bf16x8 xf[2][3];
      s16x4 wlo[4], whi[4];
      auto waddr = [&](const char* pw, int i) { const int ks1 = i / 12, c1 = i % 12; return pw + wbs[c1 & 3] + (32 * ks1) * 384 + (c1 >> 2) * 128; };
      auto qphase = [&](int sx, int sw, int sxn, int swn, auto nj_, const auto& j0, const auto& j1, auto hasNext_) {
        constexpr bool hasNext = decltype(hasNext_)::value;
        constexpr int nj = decltype(nj_)::value;
        const char* px = smem + sx * SLOT; const char* pw = smem + sw * SLOT;
        const char* pxn = smem + sxn * SLOT; const char* pwn = smem + swn * SLOT;
#pragma unroll
        for (int i = 0; i < 24; ++i) {
          const int ks = i / 12, c = i % 12;
          const bf16x8 wf = cat8(wlo[i & 3], whi[i & 3]);
          if constexpr (hasNext) { if (i == 21) { PHASE_SYNC(12); } }
          const bool ld = (i + 3 < 24) || hasNext;
          const char* an = (i + 3 < 24) ? waddr(pw, i + 3) : waddr(pwn, i + 3 - 24);
          mfma_a(acc[c][0], wf, xf[ks][0]);
          if (ld) wlo[(i + 3) & 3] = lds_tr(an);
          SB();
          mfma_a(acc[c][1], wf, xf[ks][1]);
          if (ld) whi[(i + 3) & 3] = lds_tr(an + 4 * 384);
          SB();
          mfma_a(acc[c][2], wf, xf[ks][2]);
          if (i < 3) xf[1][i] = lds_b128(px + xb1 + i * 2048);
          else if (i >= 21) { if constexpr (hasNext) xf[0][i - 21] = lds_b128(pxn + xb0 + (i - 21) * 2048); }
          else if (i % 3 != 2) { const int q = ((i - 3) / 3) * 2 + (i % 3); if (q < 6) { if constexpr (nj > 0) dma_piece(j0, q); } else { if constexpr (nj > 1) dma_piece(j1, q - 6); } }
          SB();
        }
      };
      constexpr std::integral_constant<int, 1> J1{}; constexpr std::integral_constant<int, 2> J2{};
      if (unit == 0) { PHASE_SYNC(24); } else { PHASE_SYNC(36); }
#pragma unroll
      for (int i = 0; i < 3; ++i) xf[0][i] = lds_b128(smem + ax * SLOT + xb0 + i * 2048);
#pragma unroll
      for (int i = 0; i < 3; ++i) { const char* a = waddr(smem + aw * SLOT, i); wlo[i] = lds_tr(a); whi[i] = lds_tr(a + 4 * 384); }
      qphase(ax, aw, bx, bw, J1, JOB_W(unit, 2, cw), DUMMY, T_);
      for (int i = 1; i < 8; ++i) {
        qphase(bx, bw, cx, cw, J2, JOB_X(i + 2, ax), JOB_W(unit, i + 2, aw), T_);
        { const int tx = ax, tw = aw; ax = bx; aw = bw; bx = cx; bw = cw; cx = tx; cw = tw; }
      }
      qphase(bx, bw, cx, cw, J2, JOB_K(8 * unit, ax), JOB_K(8 * unit + 1, aw), T_);
      qphase(cx, cw, cx, cw, J2, JOB_K(8 * unit + 2, bx), JOB_V(8 * unit, bw), F_);

      MFMA_DRAIN();
#pragma unroll
      for (int qt = 0; qt < 3; ++qt)
#pragma unroll
        for (int s = 0; s < 6; ++s) {
          asm volatile("" : "+a"(acc[2 * s][qt]), "+a"(acc[2 * s + 1][qt]));
          const f32x4 A = acc[2 * s][qt] * QSCALE, B = acc[2 * s + 1][qt] * QSCALE;
          u32x4 o = pair_fix(cvtpk(A[0], A[1]), cvtpk(A[2], A[3]), cvtpk(B[0], B[1]), cvtpk(B[2], B[3]));
          qf[qt][s] = __builtin_bit_cast(bf16x8, o);
          asm volatile("" : "+a"(qf[qt][s]));
          if (s % 3 == 2) SB();
        }
    }
#pragma unroll
    for (int c = 0; c < 12; ++c)
#pragma unroll
      for (int qt = 0; qt < 3; ++qt) { acc[c][qt] = f32x4{0.f, 0.f, 0.f, 0.f}; asm volatile("" : "+a"(acc[c][qt])); }

    float mref[3], lsum[3] = {0.f, 0.f, 0.f};
    f32x4 negm[3] = {f32x4{0.f, 0.f, 0.f, 0.f}, f32x4{0.f, 0.f, 0.f, 0.f}, f32x4{0.f, 0.f, 0.f, 0.f}};
    asm volatile("s_nop 1" : "+v"(negm[0]), "+v"(negm[1]), "+v"(negm[2]));
    f32x4 Sc[2][3];
    bf16x8 Pp[3];
    bf16x8 kf[3];
    s16x4 vlo[3], vhi[3];

    auto sm = [&](const f32x4 (&S)[2][3], bf16x8 (&P)[3], float (&ps)[3]) {
#pragma unroll
      for (int qt = 0; qt < 3; ++qt) {
        float e[8];
#pragma unroll
        for (int i = 0; i < 8; ++i) e[i] = __builtin_amdgcn_exp2f(S[i >> 2][qt][i & 3]);
        ps[qt] = ((e[0] + e[1]) + (e[2] + e[3])) + ((e[4] + e[5]) + (e[6] + e[7]));
        u32x4 o = {cvtpk(e[0], e[1]), cvtpk(e[2], e[3]), cvtpk(e[4], e[5]), cvtpk(e[6], e[7])};
        P[qt] = __builtin_bit_cast(bf16x8, o);
      }
    };
    auto rowmax = [&](const f32x4 (&S)[2][3], int qt) {
      float mx = fmaxf(fmaxf(fmaxf(S[0][qt][0], S[0][qt][1]), fmaxf(S[0][qt][2], S[0][qt][3])),
                       fmaxf(fmaxf(S[1][qt][0], S[1][qt][1]), fmaxf(S[1][qt][2], S[1][qt][3])));
      mx = fmaxf(mx, __shfl_xor(mx, 16));
      mx = fmaxf(mx, __shfl_xor(mx, 32));
      return mx;
    };
    auto kaddr = [&](const char* pk, int i) { const int s1 = i >> 1, k21 = i & 1; return pk + ((s1 & 1) ? kb1 : kb0) + k21 * 6144 + (s1 >> 1) * 128; };
    auto vaddr = [&](const char* pv, int c1) { return pv + vbs[c1 & 3] + (c1 >> 2) * 128; };
    auto kbase = [&](int slot, int hb) { return (const char*)smem + slot * SLOT + hb * (2 * 6144); };
    auto vbase = [&](int slot, int ks) { return (const char*)smem + slot * SLOT + (32 * ks) * 384; };
    auto step = [&](auto doQK_, const char* pk, auto doPV_, const char* pvv, auto doSM_, auto hasJob_, const auto& jb,
                    auto nQK_, const char* pkn, auto nPV_, const char* pvn, auto hasSync_) {
      constexpr bool hasJob = decltype(hasJob_)::value;
      constexpr bool doQK = decltype(doQK_)::value, doPV = decltype(doPV_)::value, doSM = decltype(doSM_)::value;
      constexpr bool nQK = decltype(nQK_)::value, nPV = decltype(nPV_)::value, hasSync = decltype(hasSync_)::value;
      f32x4 Sn[2][3];
      u32x4 Pn[3];
      float ps[3] = {0.f, 0.f, 0.f};
      float ea = 0.f, eb = 0.f;
#pragma unroll
      for (int i = 0; i < 12; ++i) {
        const int s = i >> 1, k2 = i & 1, ri = i % 3, rn = (i + 2) % 3;
        if constexpr (hasSync) { if (i == 10) { PHASE_SYNC(12); } }
        const bool in = (i + 2 < 12);
        const bool ldK = in ? doQK : nQK, ldV = in ? doPV : nPV;
        const char* ka = in ? kaddr(pk, i + 2) : kaddr(pkn, i + 2 - 12);
        const char* va = in ? vaddr(pvv, i + 2) : vaddr(pvn, i + 2 - 12);
        const bf16x8 kc = kf[ri];
        const bf16x8 vc = cat8(vlo[ri], vhi[ri]);
        const int pq = (i - 1) >> 2, pj = (i - 1) & 3;
        const int eq = i >> 2, e0 = (i & 3) * 2;
        if constexpr (doQK) { if (s == 0) mfma_v0(Sn[k2][0], kc, qf[0][s], negm[0]); else mfma_v(Sn[k2][0], kc, qf[0][s]); }
        if (ldV) vlo[rn] = lds_tr(va);
        if constexpr (doSM) { if (i > 0) { unsigned pk2 = cvtpk(ea, eb); asm volatile("" : "+v"(pk2)); Pn[pq][pj] = pk2; } }
        SB();
        if constexpr (doQK) { if (s == 0) mfma_v0(Sn[k2][1], kc, qf[1][s], negm[1]); else mfma_v(Sn[k2][1], kc, qf[1][s]); }
        if (ldV) vhi[rn] = lds_tr(va + 16 * 384);
        if constexpr (doSM) { if (i > 0) { if (pj == 0) ps[pq] = fadd_s(ea, eb); else ps[pq] = fadd_s(ps[pq], ea); } }
        SB();
        if constexpr (doQK) { if (s == 0) mfma_v0(Sn[k2][2], kc, qf[2][s], negm[2]); else mfma_v(Sn[k2][2], kc, qf[2][s]); }
        if (ldK) kf[rn] = lds_b128(ka);
        if constexpr (doSM) { if (i > 0) { if (pj != 0) ps[pq] = fadd_s(ps[pq], eb); } }
        SB();
        if constexpr (doPV) mfma_a(acc[i][0], vc, Pp[0]);
        if constexpr (doSM) { ea = __builtin_amdgcn_exp2f(Sc[e0 >> 2][eq][e0 & 3]); asm volatile("" : "+v"(ea)); }
        SB();
        if constexpr (doPV) mfma_a(acc[i][1], vc, Pp[1]);
        if constexpr (doSM) { eb = __builtin_amdgcn_exp2f(Sc[e0 >> 2][eq][(e0 & 3) + 1]); asm volatile("" : "+v"(eb)); }
        SB();
        if constexpr (doPV) mfma_a(acc[i][2], vc, Pp[2]);
        if constexpr (hasJob) { if (i < 10) { if ((i & 1) == 0) dma_piece(jb, i >> 1); else if (i == 9) dma_piece(jb, 5); } }
        if constexpr (doQK) { if (i == 2) asm volatile("" :: "v"(negm[0]), "v"(negm[1]), "v"(negm[2])); }
        SB();
      }
      if constexpr (doSM) {
        Pn[2][3] = cvtpk(ea, eb);
        ps[2] = fadd_s(ps[2], ea); ps[2] = fadd_s(ps[2], eb);
        const bool bad = !(ps[0] <= 1.0e12f) || !(ps[1] <= 1.0e12f) || !(ps[2] <= 1.0e12f);
        bf16x8 Pb[3];
#pragma unroll
        for (int qt = 0; qt < 3; ++qt) Pb[qt] = __builtin_bit_cast(bf16x8, Pn[qt]);
        if (__builtin_expect(__any(bad), 0)) {
          MFMA_DRAIN();
          if constexpr (doQK) asm volatile("" : "+v"(Sn[0][0]), "+v"(Sn[0][1]), "+v"(Sn[0][2]), "+v"(Sn[1][0]), "+v"(Sn[1][1]), "+v"(Sn[1][2]));
#pragma unroll
          for (int qt = 0; qt < 3; ++qt) {
            const float d = fmaxf(rowmax(Sc, qt), 0.f);
            const float f = __builtin_amdgcn_exp2f(-d);
            mref[qt] += d;
            negm[qt] = f32x4{-mref[qt], -mref[qt], -mref[qt], -mref[qt]};
            asm volatile("s_nop 1" : "+v"(negm[qt]));
            lsum[qt] *= f;
#pragma unroll
            for (int c = 0; c < 12; ++c) { asm volatile("" : "+a"(acc[c][qt])); acc[c][qt] *= f; asm volatile("" : "+a"(acc[c][qt])); SB(); }
#pragma unroll
            for (int k2 = 0; k2 < 2; ++k2) { Sc[k2][qt] -= d; if constexpr (doQK) Sn[k2][qt] -= d; }
          }
          sm(Sc, Pb, ps);
        }
#pragma unroll
        for (int qt = 0; qt < 3; ++qt) { lsum[qt] += ps[qt]; Pp[qt] = Pb[qt]; asm volatile("s_nop 0" : "+v"(Pp[qt])); }
      }
      if constexpr (doQK) {
#pragma unroll
        for (int qt = 0; qt < 3; ++qt) { Sc[0][qt] = Sn[0][qt]; Sc[1][qt] = Sn[1][qt]; }
      }
    };

    const int G0 = unit * 8;
    int kc = ax, kn = aw, kp = bx;
    int vn = bw, vp = cx, vc = cw;
#define ROTK() do { const int x_ = kp; kp = kc; kc = kn; kn = x_; } while (0)
#define ROTV() do { const int x_ = vp; vp = vc; vc = vn; vn = x_; } while (0)
    PHASE_SYNC(18);
    {
      kf[0] = lds_b128(kaddr(kbase(kc, 0), 0)); kf[1] = lds_b128(kaddr(kbase(kc, 0), 1));
      step(T_, kbase(kc, 0), F_, smem, F_, T_, JOB_V(G0 + 1, vp), T_, kbase(kc, 1), F_, smem, F_);
      asm volatile("s_nop 7\n\ts_nop 7" : "+v"(Sc[0][0]), "+v"(Sc[0][1]), "+v"(Sc[0][2]), "+v"(Sc[1][0]), "+v"(Sc[1][1]), "+v"(Sc[1][2]));
#pragma unroll
      for (int qt = 0; qt < 3; ++qt) {
        const float mx = rowmax(Sc, qt);
        mref[qt] = mx;
        negm[qt] = f32x4{-mx, -mx, -mx, -mx};
        asm volatile("s_nop 1" : "+v"(negm[qt]));
        Sc[0][qt] -= mx; Sc[1][qt] -= mx;
      }
      step(T_, kbase(kc, 1), F_, smem, T_, T_, JOB_V(G0 + 2, vc), T_, kbase(kn, 0), T_, vbase(vn, 0), T_);
      ROTK(); ROTV();
      step(T_, kbase(kc, 0), T_, vbase(vc, 0), T_, T_, JOB_K(G0 + 3, kp), T_, kbase(kc, 1), T_, vbase(vc, 1), F_);
      step(T_, kbase(kc, 1), T_, vbase(vc, 1), T_, F_, DUMMY, T_, kbase(kn, 0), T_, vbase(vn, 0), T_);
      ROTK(); ROTV();
    }
    for (int t = 2; t < 6; ++t) {
      step(T_, kbase(kc, 0), T_, vbase(vc, 0), T_, T_, JOB_K(G0 + t + 2, kp), T_, kbase(kc, 1), T_, vbase(vc, 1), F_);
      step(T_, kbase(kc, 1), T_, vbase(vc, 1), T_, T_, JOB_V(G0 + t + 1, vp), T_, kbase(kn, 0), T_, vbase(vn, 0), T_);
      ROTK(); ROTV();
    }
    {
      ax = kp;
      step(T_, kbase(kc, 0), T_, vbase(vc, 0), T_, T_, JOB_X(0, ax), T_, kbase(kc, 1), T_, vbase(vc, 1), F_);
      step(T_, kbase(kc, 1), T_, vbase(vc, 1), T_, T_, JOB_V(G0 + 7, vp), T_, kbase(kn, 0), T_, vbase(vn, 0), T_);
      ROTK(); ROTV();
      aw = kp; bx = vp;
      step(T_, kbase(kc, 0), T_, vbase(vc, 0), T_, T_, JOB_W(1, 0, aw), T_, kbase(kc, 1), T_, vbase(vc, 1), F_);
      step(T_, kbase(kc, 1), T_, vbase(vc, 1), T_, T_, JOB_X(1, bx), F_, smem, T_, vbase(vn, 0), T_);
      ROTK(); ROTV();
      bw = kp; cx = vp; cw = vc;
      step(F_, smem, T_, vbase(vc, 0), T_, T_, JOB_W(1, 1, bw), F_, smem, T_, vbase(vc, 1), F_);
      step(F_, smem, T_, vbase(vc, 1), F_, T_, JOB_X(2, cx), F_, smem, F_, smem, F_);
    }

    MFMA_DRAIN();
    {
      u16* orow = Og + (size_t)br * MROWS * HD + ((size_t)(3 * (2 * hp + unit)) * MROWS + (b * SQ + 48 * w + r)) * 64 + 8 * g;
#pragma unroll
      for (int qt = 0; qt < 3; ++qt) {
        float lt = lsum[qt];
        lt += __shfl_xor(lt, 16);
        lt += __shfl_xor(lt, 32);
        const float inv = 1.0f / lt;
#pragma unroll
        for (int s = 0; s < 6; ++s) {
          asm volatile("" : "+a"(acc[2 * s][qt]), "+a"(acc[2 * s + 1][qt]));
          const f32x4 A = acc[2 * s][qt] * inv, B = acc[2 * s + 1][qt] * inv;
          u32x4 o = pair_fix(cvtpk(A[0], A[1]), cvtpk(A[2], A[3]), cvtpk(B[0], B[1]), cvtpk(B[2], B[3]));
          *reinterpret_cast<u32x4*>(orow + (size_t)(s >> 1) * MROWS * 64 + qt * 16 * 64 + 32 * (s & 1)) = o;
          if (s % 3 == 2) SB();
        }
      }
    }
    STAMP();
  }
  asm volatile("s_waitcnt vmcnt(0)" ::: "memory");
}

typedef __attribute__((ext_vector_type(16))) float f32x16;
constexpr int K2_STAGE = 24576 + 16384;
constexpr int K2_LDS = 3 * K2_STAGE;
constexpr int K2_NST = 32;
#define K2_PF 5
constexpr size_t WS_O_BYTES = (size_t)2 * MROWS * HD * 2;
constexpr size_t WS_PART_OFF = WS_O_BYTES;
constexpr size_t WS_PART_BYTES = (size_t)80 * 3 * 4 * 24576;
constexpr size_t WS_CNT_OFF = WS_PART_OFF + WS_PART_BYTES;
DI void mfma32_a(f32x16& c, bf16x8 a, bf16x8 b) { asm volatile("v_mfma_f32_32x32x16_bf16 %0, %1, %2, %0" : "+a"(c) : "v"(a), "v"(b)); }

__global__ __launch_bounds__(320, 1) void k2_oproj(const u16* __restrict__ Og, const u16* __restrict__ woA, const u16* __restrict__ woB,
                                                   u16* __restrict__ out, float* __restrict__ part, unsigned* __restrict__ cnt) {
  extern __shared__ __attribute__((aligned(16))) char smem[];
  const int tid = threadIdx.x, l = tid & 63;
  const int w = __builtin_amdgcn_readfirstlane(tid >> 6);
  const int r32 = l & 31, h = l >> 5, blk = (l >> 4) & 1, q4 = (l & 15) >> 2, p4 = l & 3;
  const int mw = (w >> 1) & 1, nw = w & 1;
  const int wg = blockIdx.x, item = (wg & 7) * 30 + (wg >> 3);
  const int br = item / 120, sk = (item / 40) % 3, mt = (item % 40) / 5, nt = item % 5;
  const int tl = br * 40 + mt * 5 + nt;
  const u16* wo = br ? woB : woA;
  const __amdgpu_buffer_rsrc_t rO = __builtin_amdgcn_make_buffer_rsrc((void*)(Og + (size_t)br * MROWS * HD), 0, MROWS * HD * 2, 0x00020000);
  const __amdgpu_buffer_rsrc_t rW = __builtin_amdgcn_make_buffer_rsrc((void*)wo, 0, HD * DM * 2, 0x00020000);
  const unsigned ooff = ((unsigned)(sk * K2_NST) * MROWS + (unsigned)(mt * 192)) * 128u;
  const unsigned woff = (unsigned)(sk * 2048) * (DM * 2) + (unsigned)nt * 256;
  if (w == 4) {
    const unsigned to = (l < 39 && nt * 39 + l < 192) ? (unsigned)(nt * 39 + l) * 128u : 0x7fffffffu;
    const unsigned tw = (l < 16) ? (unsigned)(mt * 8 + (l >> 1)) * (DM * 2) + (unsigned)(l & 1) * 128u : 0x7fffffffu;
    unsigned junk = 0;
    auto tch = [&](int st) {
      junk ^= __builtin_amdgcn_raw_buffer_load_b32(rO, to, ooff + st * (MROWS * 128u), 0);
      junk ^= __builtin_amdgcn_raw_buffer_load_b32(rW, tw, woff + st * (64u * DM * 2), 0);
    };
#pragma unroll
    for (int st = 2; st < 2 + K2_PF; ++st) tch(st);
    __builtin_amdgcn_s_barrier();
    for (int st = 0; st < K2_NST - 1; ++st) {
      if (st + 2 + K2_PF < K2_NST) tch(st + 2 + K2_PF);
      __builtin_amdgcn_s_barrier();
    }
    if (junk == 0x9e3779b9u) cnt[tl * 32 + 1] = junk;
    return;
  }
  unsigned vo[6], vw[4];
#pragma unroll
  for (int j = 0; j < 6; ++j) { const int p = (w * 6 + j) * 64 + l, rx = p >> 3, cx = p & 7; vo[j] = rx * 128 + (cx ^ ((rx >> 1) & 7)) * 16; }
#pragma unroll
  for (int j = 0; j < 4; ++j) { const int p = (w * 4 + j) * 64 + l, row = p >> 4, ch = p & 15; vw[j] = row * (DM * 2) + (ch ^ ((row & 3) << 2)) * 16; }
  const unsigned lbase = (unsigned)(size_t)smem;
  auto issue1 = [&](int st, int buf, int j) {
    const unsigned lb = lbase + buf * K2_STAGE;
    if (j < 6) asm volatile("s_mov_b32 m0, %0\n\ts_nop 0\n\tbuffer_load_dwordx4 %1, %2, %3 offen lds" :: "s"(lb + (w * 6 + j) * 1024u), "v"(vo[j < 6 ? j : 0]), "s"(rO), "s"(ooff + st * (MROWS * 128u)) : "memory");
    else       asm volatile("s_mov_b32 m0, %0\n\ts_nop 0\n\tbuffer_load_dwordx4 %1, %2, %3 offen lds" :: "s"(lb + 24576u + (w * 4 + j - 6) * 1024u), "v"(vw[j >= 6 ? j - 6 : 0]), "s"(rW), "s"(woff + st * (64u * DM * 2)) : "memory");
  };
  int ob[4], wbb[2];
#pragma unroll
  for (int ks = 0; ks < 4; ++ks) ob[ks] = (96 * mw + r32) * 128 + ((2 * ks + h) ^ ((r32 >> 1) & 7)) * 16;
#pragma unroll
  for (int c = 0; c < 2; ++c) wbb[c] = 24576 + (8 * h + q4) * 256 + (((2 * nw + c) ^ q4) * 64) + 32 * blk + 8 * p4;
  f32x16 acc[2][3];
#pragma unroll
  for (int c = 0; c < 2; ++c)
#pragma unroll
    for (int m = 0; m < 3; ++m) { acc[c][m] = f32x16{}; asm volatile("" : "+a"(acc[c][m])); }
  bf16x8 of[2][3];
  s16x4 wl[2][2], wh[2][2];
#pragma unroll
  for (int j = 0; j < 10; ++j) issue1(0, 0, j);
#pragma unroll
  for (int j = 0; j < 10; ++j) issue1(1, 1, j);
  asm volatile("s_waitcnt vmcnt(10)" ::: "memory");
  __builtin_amdgcn_s_barrier();
  asm volatile("" ::: "memory");
#pragma unroll
  for (int m = 0; m < 3; ++m) of[0][m] = lds_b128(smem + ob[0] + m * 4096);
#pragma unroll
  for (int c = 0; c < 2; ++c) { wl[0][c] = lds_tr(smem + wbb[c]); wh[0][c] = lds_tr(smem + wbb[c] + 4 * 256); }
  int buf = 0;
  auto stage = [&](int st, auto doIssue_, auto hasNext_, auto last2_) {
    constexpr bool doIssue = decltype(doIssue_)::value, hasNext = decltype(hasNext_)::value, last2 = decltype(last2_)::value;
    const char* pb = smem + buf * K2_STAGE;
    const int bn = (buf == 2) ? 0 : buf + 1, bp = (buf == 0) ? 2 : buf - 1;
    const char* pn = smem + bn * K2_STAGE;
#pragma unroll
    for (int ks = 0; ks < 4; ++ks) {
      const int cu = ks & 1, nx = cu ^ 1;
      if (ks == 3) {
        if constexpr (hasNext) {
          if constexpr (last2) { asm volatile("s_waitcnt vmcnt(0) lgkmcnt(0)" ::: "memory"); } else { asm volatile("s_waitcnt vmcnt(10) lgkmcnt(0)" ::: "memory"); }
          __builtin_amdgcn_s_barrier();
          asm volatile("" ::: "memory");
        }
      }
      const bool ld = (ks < 3) || hasNext;
      const char* pr = (ks < 3) ? pb : pn;
      const int kn = (ks + 1) & 3;
#pragma unroll
      for (int gq = 0; gq < 6; ++gq) {
        const int c = gq / 3, m = gq % 3;
        mfma32_a(acc[c][m], cat8(wl[cu][c], wh[cu][c]), of[cu][m]);
        if (ld) {
          if (gq == 0) { wl[nx][0] = lds_tr(pr + wbb[0] + (16 * kn) * 256); wh[nx][0] = lds_tr(pr + wbb[0] + (16 * kn + 4) * 256); }
          else if (gq < 4) of[nx][gq - 1] = lds_b128(pr + ob[kn] + (gq - 1) * 4096);
          else if (gq == 4) { wl[nx][1] = lds_tr(pr + wbb[1] + (16 * kn) * 256); wh[nx][1] = lds_tr(pr + wbb[1] + (16 * kn + 4) * 256); }
        }
        if constexpr (doIssue) { if (ks < 3) { const int sl = ks * 6 + gq; if (sl % 5 != 4 && sl < 12) issue1(st + 2, bp, sl - sl / 5); } }
        SB();
      }
    }
    buf = bn;
  };
  constexpr std::true_type T_{}; constexpr std::false_type F_{};
  for (int st = 0; st < K2_NST - 2; ++st) stage(st, T_, T_, F_);
  stage(K2_NST - 2, F_, T_, T_);
  stage(K2_NST - 1, F_, F_, F_);
  MFMA_DRAIN();
  float* slab = part + (size_t)tl * (3 * 4 * 6144);
  const __amdgpu_buffer_rsrc_t rP = __builtin_amdgcn_make_buffer_rsrc((void*)slab, 0, 3 * 4 * 24576, 0x00020000);
#pragma unroll
  for (int c = 0; c < 2; ++c)
#pragma unroll
    for (int m = 0; m < 3; ++m) {
      if (m != sk) {
        asm volatile("" : "+a"(acc[c][m]));
#pragma unroll
        for (int i = 0; i < 4; ++i) {
          const f32x4 v = {acc[c][m][4 * i], acc[c][m][4 * i + 1], acc[c][m][4 * i + 2], acc[c][m][4 * i + 3]};
          __builtin_amdgcn_raw_buffer_store_b128(__builtin_bit_cast(u32x4, v), rP, l * 16, (unsigned)((sk * 4 + w) * 24576 + (c * 3 + m) * 4096 + i * 1024), 16);
        }
      }
    }
  asm volatile("s_waitcnt vmcnt(0)" ::: "memory");
  __syncthreads();
  if (tid == 0) __hip_atomic_fetch_add(cnt + tl * 32, 1u, __ATOMIC_RELAXED, __HIP_MEMORY_SCOPE_AGENT);
  if (w == 0) {
    unsigned spins = 0;
    while (__hip_atomic_load(cnt + tl * 32, __ATOMIC_RELAXED, __HIP_MEMORY_SCOPE_AGENT) < 3u) { __builtin_amdgcn_s_sleep(1); if (++spins > (1u << 24)) break; }
  }
  __syncthreads();
  auto fin = [&](auto J_) {
    constexpr int J = decltype(J_)::value;
    u32x4 v[2][2][4];
#pragma unroll
    for (int s2 = 0; s2 < 2; ++s2) {
      const int src = s2 + (s2 >= J ? 1 : 0);
#pragma unroll
      for (int c = 0; c < 2; ++c)
#pragma unroll
        for (int i = 0; i < 4; ++i)
          v[s2][c][i] = __builtin_amdgcn_raw_buffer_load_b128(rP, l * 16, (unsigned)((src * 4 + w) * 24576 + (c * 3 + J) * 4096 + i * 1024), 16);
    }
    u16* orow = out + (size_t)(mt * 192 + 96 * mw + 32 * J + r32) * (2 * DM) + br * DM + nt * 128 + 64 * nw + 8 * h;
#pragma unroll
    for (int c = 0; c < 2; ++c) {
      asm volatile("" : "+a"(acc[c][J]));
      unsigned pk[4][2];
#pragma unroll
      for (int i = 0; i < 4; ++i) {
        const f32x4 own = {acc[c][J][4 * i], acc[c][J][4 * i + 1], acc[c][J][4 * i + 2], acc[c][J][4 * i + 3]};
        f32x4 t[3];
#pragma unroll
        for (int s3 = 0; s3 < 3; ++s3) t[s3] = (s3 == J) ? own : __builtin_bit_cast(f32x4, v[s3 - (s3 > J ? 1 : 0)][c][i]);
        const f32x4 sum = (t[0] + t[1]) + t[2];
        pk[i][0] = cvtpk(sum[0], sum[1]); pk[i][1] = cvtpk(sum[2], sum[3]);
      }
#pragma unroll
      for (int i = 0; i < 4; i += 2) {
        auto r0 = __builtin_amdgcn_permlane32_swap(pk[i][0], pk[i + 1][0], false, false);
        auto r1 = __builtin_amdgcn_permlane32_swap(pk[i][1], pk[i + 1][1], false, false);
        u32x4 o = {r0[0], r1[0], r0[1], r1[1]};
        *reinterpret_cast<u32x4*>(orow + 32 * c + 8 * i) = o;
      }
    }
  };
  if (sk == 0) fin(std::integral_constant<int, 0>{});
  else if (sk == 1) fin(std::integral_constant<int, 1>{});
  else fin(std::integral_constant<int, 2>{});
}

extern "C" void kernel_launch(void* const* d_in, const int* in_sizes, int n_in,
                              void* d_out, int out_size, void* d_ws, size_t ws_size,
                              hipStream_t stream) {
  const u16* xA = (const u16*)d_in[0]; const u16* kA = (const u16*)d_in[1]; const u16* vA = (const u16*)d_in[2];
  const u16* wqA = (const u16*)d_in[3]; const u16* woA = (const u16*)d_in[4];
  const u16* xB = (const u16*)d_in[5]; const u16* kB = (const u16*)d_in[6]; const u16* vB = (const u16*)d_in[7];
  const u16* wqB = (const u16*)d_in[8]; const u16* woB = (const u16*)d_in[9];
  u16* Og = (u16*)d_ws;
  float* part = (float*)((char*)d_ws + WS_PART_OFF);
  unsigned* cnt = (unsigned*)((char*)d_ws + WS_CNT_OFF);
  (void)hipFuncSetAttribute((const void*)k1_attn, hipFuncAttributeMaxDynamicSharedMemorySize, K1_LDS);
  (void)hipFuncSetAttribute((const void*)k2_oproj, hipFuncAttributeMaxDynamicSharedMemorySize, K2_LDS);
  hipLaunchKernelGGL(k1_attn, dim3(256), dim3(256), K1_LDS, stream, xA, kA, vA, wqA, xB, kB, vB, wqB, Og, cnt);
  hipLaunchKernelGGL(k2_oproj, dim3(240), dim3(320), K2_LDS, stream, Og, woA, woB, (u16*)d_out, part, cnt);
}
```

```cpp
#include <hip/hip_runtime.h>
#include <hip/hip_bf16.h>
#include <stdint.h>
#include <type_traits>

#define DI __device__ __forceinline__
typedef unsigned short u16;
typedef __attribute__((ext_vector_type(8))) __bf16 bf16x8;
typedef __attribute__((ext_vector_type(2))) __bf16 bf16x2;
typedef __attribute__((ext_vector_type(4))) short s16x4;
typedef __attribute__((ext_vector_type(8))) short s16x8;
typedef __attribute__((ext_vector_type(4))) float f32x4;
typedef __attribute__((ext_vector_type(2))) float f32x2;
typedef __attribute__((ext_vector_type(4))) unsigned u32x4;
typedef __attribute__((address_space(3))) void lds_void;
typedef __attribute__((address_space(3))) s16x4 lds_s16x4;

constexpr int NB = 8, SQ = 192, DM = 640, NH = 32, SK = 512, DH = 192;
constexpr int HD = NH * DH;
constexpr int MROWS = NB * SQ;
constexpr int SLOT = 24576;
constexpr int K1_LDS = 160 * 1024;

DI unsigned cvtpk(float lo, float hi) {
  f32x2 v = {lo, hi};
  bf16x2 b = __builtin_convertvector(v, bf16x2);
  return __builtin_bit_cast(unsigned, b);
}
DI f32x4 mfma16(bf16x8 a, bf16x8 b, f32x4 c) { return __builtin_amdgcn_mfma_f32_16x16x32_bf16(a, b, c, 0, 0, 0); }
DI void mfma_a(f32x4& c, bf16x8 a, bf16x8 b) { asm volatile("v_mfma_f32_16x16x32_bf16 %0, %1, %2, %0" : "+a"(c) : "v"(a), "v"(b)); }
DI void mfma_v(f32x4& c, bf16x8 a, bf16x8 b) { asm volatile("v_mfma_f32_16x16x32_bf16 %0, %1, %2, %0" : "+v"(c) : "v"(a), "a"(b)); }
DI void mfma_vv(f32x4& c, bf16x8 a, bf16x8 b) { asm volatile("v_mfma_f32_16x16x32_bf16 %0, %1, %2, %0" : "+v"(c) : "v"(a), "v"(b)); }
DI void mfma_v0(f32x4& d, bf16x8 a, bf16x8 b, f32x4 c) { asm volatile("v_mfma_f32_16x16x32_bf16 %0, %1, %2, %3" : "=&v"(d) : "v"(a), "a"(b), "v"(c)); }
DI float fadd_s(float a, float b) { float r = a + b; asm("" : "+v"(r)); return r; }
#define MFMA_DRAIN() asm volatile("s_nop 7\n\ts_nop 7" ::: "memory")
DI bf16x8 lds_b128(const char* p) { return *reinterpret_cast<const bf16x8*>(p); }
DI bf16x8 lds_tr2(const char* p0, const char* p1) {
  s16x4 lo = __builtin_amdgcn_ds_read_tr16_b64_v4i16((lds_s16x4*)p0);
  s16x4 hi = __builtin_amdgcn_ds_read_tr16_b64_v4i16((lds_s16x4*)p1);
  s16x8 v = __builtin_shufflevector(lo, hi, 0, 1, 2, 3, 4, 5, 6, 7);
  return __builtin_bit_cast(bf16x8, v);
}
DI s16x4 lds_tr(const char* p) { return __builtin_amdgcn_ds_read_tr16_b64_v4i16((lds_s16x4*)p); }
DI bf16x8 cat8(s16x4 lo, s16x4 hi) { s16x8 v = __builtin_shufflevector(lo, hi, 0, 1, 2, 3, 4, 5, 6, 7); return __builtin_bit_cast(bf16x8, v); }
#define SB() __builtin_amdgcn_sched_barrier(0)
#define WAITV(n) asm volatile("s_waitcnt vmcnt(" #n ")" ::: "memory")
#define STAMP() do {} while (0)
#define PHASE_SYNC(n) do { STAMP(); asm volatile("s_waitcnt vmcnt(" #n ") lgkmcnt(0)" ::: "memory"); STAMP(); __builtin_amdgcn_s_barrier(); asm volatile("" ::: "memory"); STAMP(); } while (0)

DI u32x4 pair_fix(unsigned a0, unsigned a1, unsigned b0, unsigned b1) {
  auto r0 = __builtin_amdgcn_permlane32_swap(a0, b0, false, false);
  auto r1 = __builtin_amdgcn_permlane32_swap(a1, b1, false, false);
  auto s0 = __builtin_amdgcn_permlane16_swap(r0[0], r0[1], false, false);
  auto s1 = __builtin_amdgcn_permlane16_swap(r1[0], r1[1], false, false);
  u32x4 o = {s0[0], s1[0], s0[1], s1[1]};
  return o;
}

struct DmaOff { unsigned k[6], v[6], w[6], x[6]; };

DI void dma_tile(__amdgpu_buffer_rsrc_t rs, const unsigned (&vo)[6], unsigned soff, char* smem, int slot, int w) {
  const unsigned lb = (unsigned)(size_t)smem + (unsigned)(slot * SLOT + w * 6144);
#pragma unroll
  for (int j = 0; j < 6; ++j)
    asm volatile("s_mov_b32 m0, %0\n\ts_nop 0\n\tbuffer_load_dwordx4 %1, %2, %3 offen lds" :: "s"(lb + j * 1024u), "v"(vo[j]), "s"(rs), "s"(soff) : "memory");
}

struct DmaJob { __amdgpu_buffer_rsrc_t rs; unsigned soff, lb; unsigned vo[6]; int valid; };
DI DmaJob mkjob(__amdgpu_buffer_rsrc_t rs, const unsigned (&vo)[6], unsigned soff, char* smem, int slot, int w) {
  DmaJob j; j.rs = rs; j.soff = soff; j.lb = (unsigned)(size_t)smem + (unsigned)(slot * SLOT + w * 6144); j.valid = 1;
#pragma unroll
  for (int i = 0; i < 6; ++i) j.vo[i] = vo[i];
  return j;
}
struct DmaJobNT : DmaJob {};
DI DmaJobNT mkjob_nt(__amdgpu_buffer_rsrc_t rs, const unsigned (&vo)[6], unsigned soff, char* smem, int slot, int w) { DmaJobNT j; (DmaJob&)j = mkjob(rs, vo, soff, smem, slot, w); return j; }
template <int J, bool NT> DI void dma_piece_(const DmaJob& jb) {
  if constexpr ((J & 3) == 0) asm volatile("s_mov_b32 m0, %0\n\ts_nop 0" :: "s"(jb.lb + J * 1024u) : "memory");
  if constexpr (NT) asm volatile("buffer_load_dwordx4 %0, %1, %2 offen offset:%3 nt lds" :: "v"(jb.vo[J]), "s"(jb.rs), "s"(jb.soff), "i"((J & 3) * 1024) : "memory");
  else              asm volatile("buffer_load_dwordx4 %0, %1, %2 offen offset:%3 lds" :: "v"(jb.vo[J]), "s"(jb.rs), "s"(jb.soff), "i"((J & 3) * 1024) : "memory");
}
template <bool NT> DI void dma_piece_sw(const DmaJob& jb, int j) {
  switch (j) { case 0: dma_piece_<0, NT>(jb); break; case 1: dma_piece_<1, NT>(jb); break; case 2: dma_piece_<2, NT>(jb); break;
               case 3: dma_piece_<3, NT>(jb); break; case 4: dma_piece_<4, NT>(jb); break; default: dma_piece_<5, NT>(jb); break; }
}
DI void dma_piece(const DmaJobNT& jb, int j) { dma_piece_sw<true>(jb, j); }
DI void fixjob(DmaJob& j) { j.soff = __builtin_amdgcn_readfirstlane(j.soff); j.lb = __builtin_amdgcn_readfirstlane(j.lb); }
DI void dma_piece(const DmaJob& jb, int j) { dma_piece_sw<false>(jb, j); }

__global__ __launch_bounds__(256, 1) void k1_attn(const u16* __restrict__ xA, const u16* __restrict__ kA, const u16* __restrict__ vA, const u16* __restrict__ wqA,
                                                  const u16* __restrict__ xB, const u16* __restrict__ kB, const u16* __restrict__ vB, const u16* __restrict__ wqB,
                                                  u16* __restrict__ Og, unsigned* __restrict__ cnt) {
  extern __shared__ __attribute__((aligned(16))) char smem[];
  const int tid = threadIdx.x, l = tid & 63;
  const int w = __builtin_amdgcn_readfirstlane(tid >> 6);
  const int r = l & 15, g = l >> 4, q4 = (l & 15) >> 2, p4 = l & 3;
  const int wg = blockIdx.x, xcd = wg & 7, idx = wg >> 3;
  const int br = xcd >> 2, hp = (xcd & 3) * 4 + (idx & 3), b = idx >> 2;

  if (blockIdx.x == 0 && tid < 80) cnt[tid * 32] = 0u;
  DmaOff D;
#pragma unroll
  for (int j = 0; j < 6; ++j) {
    const int p = (w * 6 + j) * 64 + l;
    const int row = p / 24, chp = p % 24;
    D.k[j] = row * 384 + ((chp & ~7) | ((chp & 7) ^ ((row >> 1) & 7))) * 16;
    D.v[j] = row * 384 + ((chp & ~7) | ((chp & 7) ^ (((row >> 1) & 3) << 1))) * 16;
    D.w[j] = row * 12288 + ((chp & ~7) | ((chp & 7) ^ ((((row >> 1) & 1) << 1) | (((row >> 3) & 1) << 2)))) * 16;
    const int rx = p >> 3, cx = p & 7;
    D.x[j] = rx * 1280 + (cx ^ ((rx >> 1) & 7)) * 16;
    const unsigned bias = 3072u - 1024u * (j & 3);
    D.k[j] += bias; D.v[j] += bias; D.w[j] += bias; D.x[j] += bias;
  }
  const int kb0 = r * 384 + ((g) ^ (r >> 1)) * 16;
  const int kb1 = r * 384 + ((4 + g) ^ (r >> 1)) * 16;
  int vbs[4], wbs[4];
  {
    const int fv = ((2 * g + (q4 >> 1)) & 3) << 1;
    const int fw = ((q4 >> 1) << 1) | ((g & 1) << 2);
#pragma unroll
    for (int c = 0; c < 4; ++c) {
      vbs[c] = (4 * g + q4) * 384 + (((2 * c) ^ fv) + (p4 >> 1)) * 16 + 8 * (p4 & 1);
      wbs[c] = (8 * g + q4) * 384 + (((2 * c) ^ fw) + (p4 >> 1)) * 16 + 8 * (p4 & 1);
    }
  }
  const int xb0 = (48 * w + r) * 128 + ((g) ^ (r >> 1)) * 16;
  const int xb1 = (48 * w + r) * 128 + ((4 + g) ^ (r >> 1)) * 16;

  const unsigned xoff = (unsigned)b * (SQ * DM * 2);
  const unsigned woff = (unsigned)hp * (2 * DH * 2);
  const unsigned kvoff = (unsigned)(b * NH + 2 * hp) * (SK * DH * 2);

  const __amdgpu_buffer_rsrc_t rX = __builtin_amdgcn_make_buffer_rsrc((void*)((const char*)(br ? xB : xA) - 3072), 0, NB * SQ * DM * 2 + 3072, 0x00020000);
  const __amdgpu_buffer_rsrc_t rW = __builtin_amdgcn_make_buffer_rsrc((void*)((const char*)(br ? wqB : wqA) - 3072), 0, DM * HD * 2 + 3072, 0x00020000);
  const __amdgpu_buffer_rsrc_t rK = __builtin_amdgcn_make_buffer_rsrc((void*)((const char*)(br ? kB : kA) - 3072), 0, NB * NH * SK * DH * 2 + 3072, 0x00020000);
  const __amdgpu_buffer_rsrc_t rV = __builtin_amdgcn_make_buffer_rsrc((void*)((const char*)(br ? vB : vA) - 3072), 0, NB * NH * SK * DH * 2 + 3072, 0x00020000);

#define JOB_X(i, slot) mkjob(rX, D.x, xoff + (i) * 128u, smem, slot, w)
#define JOB_W(hh, i, slot) mkjob(rW, D.w, woff + (hh) * 384u + (i) * (64u * 12288u), smem, slot, w)
#define JOB_K(t, slot) mkjob_nt(rK, D.k, kvoff + (t) * 24576u, smem, slot, w)
#define JOB_V(t, slot) mkjob_nt(rV, D.v, kvoff + (t) * 24576u, smem, slot, w)
#define ISSUE(job) do { const DmaJob j_ = (job); _Pragma("unroll") for (int q_ = 0; q_ < 6; ++q_) dma_piece(j_, q_); } while (0)
  const DmaJob DUMMY = JOB_X(0, 0);

  ISSUE(JOB_X(0, 0)); ISSUE(JOB_W(0, 0, 1)); ISSUE(JOB_X(1, 2)); ISSUE(JOB_W(0, 1, 3)); ISSUE(JOB_X(2, 4)); ISSUE(JOB_W(0, 2, 5));

  const float QSCALE = 0.07216878364870322f * 1.4426950408889634f;
  constexpr std::true_type T_{}; constexpr std::false_type F_{};
  int ax = 0, aw = 1, bx = 2, bw = 3, cx = 4, cw = 5;

  for (int unit = 0; unit < 2; ++unit) {
    f32x4 acc[12][3];
    bf16x8 qf[3][6];
#pragma unroll
    for (int c = 0; c < 12; ++c)
#pragma unroll
      for (int qt = 0; qt < 3; ++qt) { acc[c][qt] = f32x4{0.f, 0.f, 0.f, 0.f}; asm volatile("" : "+a"(acc[c][qt])); }
    {
      bf16x8 xf[2][3];
      s16x4 wlo[4], whi[4];
      auto waddr = [&](const char* pw, int i) { const int ks1 = i / 12, c1 = i % 12; return pw + wbs[c1 & 3] + (32 * ks1) * 384 + (c1 >> 2) * 128; };
      auto qphase = [&](int sx, int sw, int sxn, int swn, auto nj_, const auto& j0, const auto& j1, auto hasNext_) {
        constexpr bool hasNext = decltype(hasNext_)::value;
        constexpr int nj = decltype(nj_)::value;
        const char* px = smem + sx * SLOT; const char* pw = smem + sw * SLOT;
        const char* pxn = smem + sxn * SLOT; const char* pwn = smem + swn * SLOT;
#pragma unroll
        for (int i = 0; i < 24; ++i) {
          const int ks = i / 12, c = i % 12;
          const bf16x8 wf = cat8(wlo[i & 3], whi[i & 3]);
          if constexpr (hasNext) { if (i == 21) { PHASE_SYNC(12); } }
          const bool ld = (i + 3 < 24) || hasNext;
          const char* an = (i + 3 < 24) ? waddr(pw, i + 3) : waddr(pwn, i + 3 - 24);
          mfma_a(acc[c][0], wf, xf[ks][0]);
          if (ld) wlo[(i + 3) & 3] = lds_tr(an);
          SB();
          mfma_a(acc[c][1], wf, xf[ks][1]);
          if (ld) whi[(i + 3) & 3] = lds_tr(an + 4 * 384);
          SB();
          mfma_a(acc[c][2], wf, xf[ks][2]);
          if (i < 3) xf[1][i] = lds_b128(px + xb1 + i * 2048);
          else if (i >= 21) { if constexpr (hasNext) xf[0][i - 21] = lds_b128(pxn + xb0 + (i - 21) * 2048); }
          else if (i % 3 != 2) { const int q = ((i - 3) / 3) * 2 + (i % 3); if (q < 6) { if constexpr (nj > 0) dma_piece(j0, q); } else { if constexpr (nj > 1) dma_piece(j1, q - 6); } }
          SB();
        }
      };
      constexpr std::integral_constant<int, 1> J1{}; constexpr std::integral_constant<int, 2> J2{};
      if (unit == 0) { PHASE_SYNC(24); } else { PHASE_SYNC(36); }
#pragma unroll
      for (int i = 0; i < 3; ++i) xf[0][i] = lds_b128(smem + ax * SLOT + xb0 + i * 2048);
#pragma unroll
      for (int i = 0; i < 3; ++i) { const char* a = waddr(smem + aw * SLOT, i); wlo[i] = lds_tr(a); whi[i] = lds_tr(a + 4 * 384); }
      qphase(ax, aw, bx, bw, J1, JOB_W(unit, 2, cw), DUMMY, T_);
      for (int i = 1; i < 8; ++i) {
        qphase(bx, bw, cx, cw, J2, JOB_X(i + 2, ax), JOB_W(unit, i + 2, aw), T_);
        { const int tx = ax, tw = aw; ax = bx; aw = bw; bx = cx; bw = cw; cx = tx; cw = tw; }
      }
      qphase(bx, bw, cx, cw, J2, JOB_K(8 * unit, ax), JOB_K(8 * unit + 1, aw), T_);
      qphase(cx, cw, cx, cw, J2, JOB_K(8 * unit + 2, bx), JOB_V(8 * unit, bw), F_);

      MFMA_DRAIN();
#pragma unroll
      for (int qt = 0; qt < 3; ++qt)
#pragma unroll
        for (int s = 0; s < 6; ++s) {
          asm volatile("" : "+a"(acc[2 * s][qt]), "+a"(acc[2 * s + 1][qt]));
          const f32x4 A = acc[2 * s][qt] * QSCALE, B = acc[2 * s + 1][qt] * QSCALE;
          u32x4 o = pair_fix(cvtpk(A[0], A[1]), cvtpk(A[2], A[3]), cvtpk(B[0], B[1]), cvtpk(B[2], B[3]));
          qf[qt][s] = __builtin_bit_cast(bf16x8, o);
          asm volatile("" : "+a"(qf[qt][s]));
          if (s % 3 == 2) SB();
        }
    }
#pragma unroll
    for (int c = 0; c < 12; ++c)
#pragma unroll
      for (int qt = 0; qt < 3; ++qt) { acc[c][qt] = f32x4{0.f, 0.f, 0.f, 0.f}; asm volatile("" : "+a"(acc[c][qt])); }

    float mref[3], lsum[3] = {0.f, 0.f, 0.f};
    f32x4 negm[3] = {f32x4{0.f, 0.f, 0.f, 0.f}, f32x4{0.f, 0.f, 0.f, 0.f}, f32x4{0.f, 0.f, 0.f, 0.f}};
    asm volatile("s_nop 1" : "+v"(negm[0]), "+v"(negm[1]), "+v"(negm[2]));
    f32x4 Sc[2][3];
    bf16x8 Pp[3];
    bf16x8 kf[3];
    s16x4 vlo[3], vhi[3];

    auto sm = [&](const f32x4 (&S)[2][3], bf16x8 (&P)[3], float (&ps)[3]) {
#pragma unroll
      for (int qt = 0; qt < 3; ++qt) {
        float e[8];
#pragma unroll
        for (int i = 0; i < 8; ++i) e[i] = __builtin_amdgcn_exp2f(S[i >> 2][qt][i & 3]);
        ps[qt] = ((e[0] + e[1]) + (e[2] + e[3])) + ((e[4] + e[5]) + (e[6] + e[7]));
        u32x4 o = {cvtpk(e[0], e[1]), cvtpk(e[2], e[3]), cvtpk(e[4], e[5]), cvtpk(e[6], e[7])};
        P[qt] = __builtin_bit_cast(bf16x8, o);
      }
    };
    auto rowmax = [&](const f32x4 (&S)[2][3], int qt) {
      float mx = fmaxf(fmaxf(fmaxf(S[0][qt][0], S[0][qt][1]), fmaxf(S[0][qt][2], S[0][qt][3])),
                       fmaxf(fmaxf(S[1][qt][0], S[1][qt][1]), fmaxf(S[1][qt][2], S[1][qt][3])));
      mx = fmaxf(mx, __shfl_xor(mx, 16));
      mx = fmaxf(mx, __shfl_xor(mx, 32));
      return mx;
    };
    auto kaddr = [&](const char* pk, int i) { const int s1 = i >> 1, k21 = i & 1; return pk + ((s1 & 1) ? kb1 : kb0) + k21 * 6144 + (s1 >> 1) * 128; };
    auto vaddr = [&](const char* pv, int c1) { return pv + vbs[c1 & 3] + (c1 >> 2) * 128; };
    auto kbase = [&](int slot, int hb) { return (const char*)smem + slot * SLOT + hb * (2 * 6144); };
    auto vbase = [&](int slot, int ks) { return (const char*)smem + slot * SLOT + (32 * ks) * 384; };
    auto step = [&](auto doQK_, const char* pk, auto doPV_, const char* pvv, auto doSM_, auto hasJob_, const auto& jb,
                    auto nQK_, const char* pkn, auto nPV_, const char* pvn, auto hasSync_) {
      constexpr bool hasJob = decltype(hasJob_)::value;
      constexpr bool doQK = decltype(doQK_)::value, doPV = decltype(doPV_)::value, doSM = decltype(doSM_)::value;
      constexpr bool nQK = decltype(nQK_)::value, nPV = decltype(nPV_)::value, hasSync = decltype(hasSync_)::value;
      f32x4 Sn[2][3];
      u32x4 Pn[3];
      float ps[3] = {0.f, 0.f, 0.f};
      float ea = 0.f, eb = 0.f;
#pragma unroll
      for (int i = 0; i < 12; ++i) {
        const int s = i >> 1, k2 = i & 1, ri = i % 3, rn = (i + 2) % 3;
        if constexpr (hasSync) { if (i == 10) { PHASE_SYNC(12); } }
        const bool in = (i + 2 < 12);
        const bool ldK = in ? doQK : nQK, ldV = in ? doPV : nPV;
        const char* ka = in ? kaddr(pk, i + 2) : kaddr(pkn, i + 2 - 12);
        const char* va = in ? vaddr(pvv, i + 2) : vaddr(pvn, i + 2 - 12);
        const bf16x8 kc = kf[ri];
        const bf16x8 vc = cat8(vlo[ri], vhi[ri]);
        const int pq = (i - 1) >> 2, pj = (i - 1) & 3;
        const int eq = i >> 2, e0 = (i & 3) * 2;
        if constexpr (doQK) { if (s == 0) mfma_v0(Sn[k2][0], kc, qf[0][s], negm[0]); else mfma_v(Sn[k2][0], kc, qf[0][s]); }
        if (ldV) vlo[rn] = lds_tr(va);
        if constexpr (doSM) { if (i > 0) { unsigned pk2 = cvtpk(ea, eb); asm volatile("" : "+v"(pk2)); Pn[pq][pj] = pk2; } }
        SB();
        if constexpr (doQK) { if (s == 0) mfma_v0(Sn[k2][1], kc, qf[1][s], negm[1]); else mfma_v(Sn[k2][1], kc, qf[1][s]); }
        if (ldV) vhi[rn] = lds_tr(va + 16 * 384);
        if constexpr (doSM) { if (i > 0) { if (pj == 0) ps[pq] = fadd_s(ea, eb); else ps[pq] = fadd_s(ps[pq], ea); } }
        SB();
        if constexpr (doQK) { if (s == 0) mfma_v0(Sn[k2][2], kc, qf[2][s], negm[2]); else mfma_v(Sn[k2][2], kc, qf[2][s]); }
        if (ldK) kf[rn] = lds_b128(ka);
        if constexpr (doSM) { if (i > 0) { if (pj != 0) ps[pq] = fadd_s(ps[pq], eb); } }
        SB();
        if constexpr (doPV) mfma_a(acc[i][0], vc, Pp[0]);
        if constexpr (doSM) { ea = __builtin_amdgcn_exp2f(Sc[e0 >> 2][eq][e0 & 3]); asm volatile("" : "+v"(ea)); }
        SB();
        if constexpr (doPV) mfma_a(acc[i][1], vc, Pp[1]);
        if constexpr (doSM) { eb = __builtin_amdgcn_exp2f(Sc[e0 >> 2][eq][(e0 & 3) + 1]); asm volatile("" : "+v"(eb)); }
        SB();
        if constexpr (doPV) mfma_a(acc[i][2], vc, Pp[2]);
        if constexpr (hasJob) { if (i < 10) { if ((i & 1) == 0) dma_piece(jb, i >> 1); else if (i == 9) dma_piece(jb, 5); } }
        if constexpr (doQK) { if (i == 2) asm volatile("" :: "v"(negm[0]), "v"(negm[1]), "v"(negm[2])); }
        SB();
      }
      if constexpr (doSM) {
        Pn[2][3] = cvtpk(ea, eb);
        ps[2] = fadd_s(ps[2], ea); ps[2] = fadd_s(ps[2], eb);
        const bool bad = !(ps[0] <= 1.0e12f) || !(ps[1] <= 1.0e12f) || !(ps[2] <= 1.0e12f);
        bf16x8 Pb[3];
#pragma unroll
        for (int qt = 0; qt < 3; ++qt) Pb[qt] = __builtin_bit_cast(bf16x8, Pn[qt]);
        if (__builtin_expect(__any(bad), 0)) {
          MFMA_DRAIN();
          if constexpr (doQK) asm volatile("" : "+v"(Sn[0][0]), "+v"(Sn[0][1]), "+v"(Sn[0][2]), "+v"(Sn[1][0]), "+v"(Sn[1][1]), "+v"(Sn[1][2]));
#pragma unroll
          for (int qt = 0; qt < 3; ++qt) {
            const float d = fmaxf(rowmax(Sc, qt), 0.f);
            const float f = __builtin_amdgcn_exp2f(-d);
            mref[qt] += d;
            negm[qt] = f32x4{-mref[qt], -mref[qt], -mref[qt], -mref[qt]};
            asm volatile("s_nop 1" : "+v"(negm[qt]));
            lsum[qt] *= f;
#pragma unroll
            for (int c = 0; c < 12; ++c) { asm volatile("" : "+a"(acc[c][qt])); acc[c][qt] *= f; asm volatile("" : "+a"(acc[c][qt])); SB(); }
#pragma unroll
            for (int k2 = 0; k2 < 2; ++k2) { Sc[k2][qt] -= d; if constexpr (doQK) Sn[k2][qt] -= d; }
          }
          sm(Sc, Pb, ps);
        }
#pragma unroll
        for (int qt = 0; qt < 3; ++qt) { lsum[qt] += ps[qt]; Pp[qt] = Pb[qt]; asm volatile("s_nop 0" : "+v"(Pp[qt])); }
      }
      if constexpr (doQK) {
#pragma unroll
        for (int qt = 0; qt < 3; ++qt) { Sc[0][qt] = Sn[0][qt]; Sc[1][qt] = Sn[1][qt]; }
      }
    };

    const int G0 = unit * 8;
    int kc = ax, kn = aw, kp = bx;
    int vn = bw, vp = cx, vc = cw;
#define ROTK() do { const int x_ = kp; kp = kc; kc = kn; kn = x_; } while (0)
#define ROTV() do { const int x_ = vp; vp = vc; vc = vn; vn = x_; } while (0)
    PHASE_SYNC(18);
    {
      kf[0] = lds_b128(kaddr(kbase(kc, 0), 0)); kf[1] = lds_b128(kaddr(kbase(kc, 0), 1));
      step(T_, kbase(kc, 0), F_, smem, F_, T_, JOB_V(G0 + 1, vp), T_, kbase(kc, 1), F_, smem, F_);
      asm volatile("s_nop 7\n\ts_nop 7" : "+v"(Sc[0][0]), "+v"(Sc[0][1]), "+v"(Sc[0][2]), "+v"(Sc[1][0]), "+v"(Sc[1][1]), "+v"(Sc[1][2]));
#pragma unroll
      for (int qt = 0; qt < 3; ++qt) {
        const float mx = rowmax(Sc, qt);
        mref[qt] = mx;
        negm[qt] = f32x4{-mx, -mx, -mx, -mx};
        asm volatile("s_nop 1" : "+v"(negm[qt]));
        Sc[0][qt] -= mx; Sc[1][qt] -= mx;
      }
      step(T_, kbase(kc, 1), F_, smem, T_, T_, JOB_V(G0 + 2, vc), T_, kbase(kn, 0), T_, vbase(vn, 0), T_);
      ROTK(); ROTV();
      step(T_, kbase(kc, 0), T_, vbase(vc, 0), T_, T_, JOB_K(G0 + 3, kp), T_, kbase(kc, 1), T_, vbase(vc, 1), F_);
      step(T_, kbase(kc, 1), T_, vbase(vc, 1), T_, F_, DUMMY, T_, kbase(kn, 0), T_, vbase(vn, 0), T_);
      ROTK(); ROTV();
    }
    for (int t = 2; t < 6; ++t) {
      step(T_, kbase(kc, 0), T_, vbase(vc, 0), T_, T_, JOB_K(G0 + t + 2, kp), T_, kbase(kc, 1), T_, vbase(vc, 1), F_);
      step(T_, kbase(kc, 1), T_, vbase(vc, 1), T_, T_, JOB_V(G0 + t + 1, vp), T_, kbase(kn, 0), T_, vbase(vn, 0), T_);
      ROTK(); ROTV();
    }
    {
      ax = kp;
      step(T_, kbase(kc, 0), T_, vbase(vc, 0), T_, T_, JOB_X(0, ax), T_, kbase(kc, 1), T_, vbase(vc, 1), F_);
      step(T_, kbase(kc, 1), T_, vbase(vc, 1), T_, T_, JOB_V(G0 + 7, vp), T_, kbase(kn, 0), T_, vbase(vn, 0), T_);
      ROTK(); ROTV();
      aw = kp; bx = vp;
      step(T_, kbase(kc, 0), T_, vbase(vc, 0), T_, T_, JOB_W(1, 0, aw), T_, kbase(kc, 1), T_, vbase(vc, 1), F_);
      step(T_, kbase(kc, 1), T_, vbase(vc, 1), T_, T_, JOB_X(1, bx), F_, smem, T_, vbase(vn, 0), T_);
      ROTK(); ROTV();
      bw = kp; cx = vp; cw = vc;
      step(F_, smem, T_, vbase(vc, 0), T_, T_, JOB_W(1, 1, bw), F_, smem, T_, vbase(vc, 1), F_);
      step(F_, smem, T_, vbase(vc, 1), F_, T_, JOB_X(2, cx), F_, smem, F_, smem, F_);
    }

    MFMA_DRAIN();
    {
      u16* orow = Og + (size_t)br * MROWS * HD + ((size_t)(3 * (2 * hp + unit)) * MROWS + (b * SQ + 48 * w + r)) * 64 + 8 * g;
#pragma unroll
      for (int qt = 0; qt < 3; ++qt) {
        float lt = lsum[qt];
        lt += __shfl_xor(lt, 16);
        lt += __shfl_xor(lt, 32);
        const float inv = 1.0f / lt;
#pragma unroll
        for (int s = 0; s < 6; ++s) {
          asm volatile("" : "+a"(acc[2 * s][qt]), "+a"(acc[2 * s + 1][qt]));
          const f32x4 A = acc[2 * s][qt] * inv, B = acc[2 * s + 1][qt] * inv;
          u32x4 o = pair_fix(cvtpk(A[0], A[1]), cvtpk(A[2], A[3]), cvtpk(B[0], B[1]), cvtpk(B[2], B[3]));
          *reinterpret_cast<u32x4*>(orow + (size_t)(s >> 1) * MROWS * 64 + qt * 16 * 64 + 32 * (s & 1)) = o;
          if (s % 3 == 2) SB();
        }
      }
    }
    STAMP();
  }
  asm volatile("s_waitcnt vmcnt(0)" ::: "memory");
}

typedef __attribute__((ext_vector_type(16))) float f32x16;
constexpr int K2_STAGE = 24576 + 16384;
constexpr int K2_LDS = 3 * K2_STAGE;
constexpr int K2_NST = 32;
#define K2_PF 5
constexpr size_t WS_O_BYTES = (size_t)2 * MROWS * HD * 2;
constexpr size_t WS_PART_OFF = WS_O_BYTES;
constexpr size_t WS_PART_BYTES = (size_t)80 * 3 * 4 * 24576;
constexpr size_t WS_CNT_OFF = WS_PART_OFF + WS_PART_BYTES;
DI void mfma32_a(f32x16& c, bf16x8 a, bf16x8 b) { asm volatile("v_mfma_f32_32x32x16_bf16 %0, %1, %2, %0" : "+a"(c) : "v"(a), "v"(b)); }

__global__ __launch_bounds__(320, 1) void k2_oproj(const u16* __restrict__ Og, const u16* __restrict__ woA, const u16* __restrict__ woB,
                                                   u16* __restrict__ out, float* __restrict__ part, unsigned* __restrict__ cnt) {
  extern __shared__ __attribute__((aligned(16))) char smem[];
  const int tid = threadIdx.x, l = tid & 63;
  const int w = __builtin_amdgcn_readfirstlane(tid >> 6);
  const int r32 = l & 31, h = l >> 5, blk = (l >> 4) & 1, q4 = (l & 15) >> 2, p4 = l & 3;
  const int mw = (w >> 1) & 1, nw = w & 1;
  const int wg = blockIdx.x, item = (wg & 7) * 30 + (wg >> 3);
  const int br = item / 120, sk = (item / 40) % 3, mt = (item % 40) / 5, nt = item % 5;
  const int tl = br * 40 + mt * 5 + nt;
  const u16* wo = br ? woB : woA;
  const __amdgpu_buffer_rsrc_t rO = __builtin_amdgcn_make_buffer_rsrc((void*)(Og + (size_t)br * MROWS * HD), 0, MROWS * HD * 2, 0x00020000);
  const __amdgpu_buffer_rsrc_t rW = __builtin_amdgcn_make_buffer_rsrc((void*)wo, 0, HD * DM * 2, 0x00020000);
  const unsigned ooff = ((unsigned)(sk * K2_NST) * MROWS + (unsigned)(mt * 192)) * 128u;
  const unsigned woff = (unsigned)(sk * 2048) * (DM * 2) + (unsigned)nt * 256;
  if (w == 4) {
    const unsigned to = (l < 39 && nt * 39 + l < 192) ? (unsigned)(nt * 39 + l) * 128u : 0x7fffffffu;
    const unsigned tw = (l < 16) ? (unsigned)(mt * 8 + (l >> 1)) * (DM * 2) + (unsigned)(l & 1) * 128u : 0x7fffffffu;
    unsigned junk = 0;
    auto tch = [&](int st) {
      junk ^= __builtin_amdgcn_raw_buffer_load_b32(rO, to, ooff + st * (MROWS * 128u), 0);
      junk ^= __builtin_amdgcn_raw_buffer_load_b32(rW, tw, woff + st * (64u * DM * 2), 0);
    };
#pragma unroll
    for (int st = 2; st < 2 + K2_PF; ++st) tch(st);
    __builtin_amdgcn_s_barrier();
    for (int st = 0; st < K2_NST - 1; ++st) {
      if (st + 2 + K2_PF < K2_NST) tch(st + 2 + K2_PF);
      __builtin_amdgcn_s_barrier();
    }
    if (junk == 0x9e3779b9u) cnt[tl * 32 + 1] = junk;
    return;
  }
  unsigned vo[6], vw[4];
#pragma unroll
  for (int j = 0; j < 6; ++j) { const int p = (w * 6 + j) * 64 + l, rx = p >> 3, cx = p & 7; vo[j] = rx * 128 + (cx ^ ((rx >> 1) & 7)) * 16 + 3072u - 1024u * (j & 3); }
#pragma unroll
  for (int j = 0; j < 4; ++j) { const int p = (w * 4 + j) * 64 + l, row = p >> 4, ch = p & 15; vw[j] = row * (DM * 2) + (ch ^ ((row & 3) << 2)) * 16 + 3072u - 1024u * (j & 3); }
  const unsigned lbase = (unsigned)(size_t)smem;
  const __amdgpu_buffer_rsrc_t rOd = __builtin_amdgcn_make_buffer_rsrc((void*)((const char*)(Og + (size_t)br * MROWS * HD) - 3072), 0, MROWS * HD * 2 + 3072, 0x00020000);
  const __amdgpu_buffer_rsrc_t rWd = __builtin_amdgcn_make_buffer_rsrc((void*)((const char*)wo - 3072), 0, HD * DM * 2 + 3072, 0x00020000);
  auto issue1 = [&](int st, int buf, int j) {
    const unsigned lb = lbase + buf * K2_STAGE;
    if (j == 0 || j == 4) asm volatile("s_mov_b32 m0, %0\n\ts_nop 0" :: "s"(lb + (w * 6 + j) * 1024u) : "memory");
    if (j == 6) asm volatile("s_mov_b32 m0, %0\n\ts_nop 0" :: "s"(lb + 24576u + (w * 4) * 1024u) : "memory");
    const unsigned so = ooff + st * (MROWS * 128u), sw = woff + st * (64u * DM * 2);
    switch (j) {
      case 0: asm volatile("buffer_load_dwordx4 %0, %1, %2 offen lds" :: "v"(vo[0]), "s"(rOd), "s"(so) : "memory"); break;
      case 1: asm volatile("buffer_load_dwordx4 %0, %1, %2 offen offset:1024 lds" :: "v"(vo[1]), "s"(rOd), "s"(so) : "memory"); break;
      case 2: asm volatile("buffer_load_dwordx4 %0, %1, %2 offen offset:2048 lds" :: "v"(vo[2]), "s"(rOd), "s"(so) : "memory"); break;
      case 3: asm volatile("buffer_load_dwordx4 %0, %1, %2 offen offset:3072 lds" :: "v"(vo[3]), "s"(rOd), "s"(so) : "memory"); break;
      case 4: asm volatile("buffer_load_dwordx4 %0, %1, %2 offen lds" :: "v"(vo[4]), "s"(rOd), "s"(so) : "memory"); break;
      case 5: asm volatile("buffer_load_dwordx4 %0, %1, %2 offen offset:1024 lds" :: "v"(vo[5]), "s"(rOd), "s"(so) : "memory"); break;
      case 6: asm volatile("buffer_load_dwordx4 %0, %1, %2 offen lds" :: "v"(vw[0]), "s"(rWd), "s"(sw) : "memory"); break;
      case 7: asm volatile("buffer_load_dwordx4 %0, %1, %2 offen offset:1024 lds" :: "v"(vw[1]), "s"(rWd), "s"(sw) : "memory"); break;
      case 8: asm volatile("buffer_load_dwordx4 %0, %1, %2 offen offset:2048 lds" :: "v"(vw[2]), "s"(rWd), "s"(sw) : "memory"); break;
      default: asm volatile("buffer_load_dwordx4 %0, %1, %2 offen offset:3072 lds" :: "v"(vw[3]), "s"(rWd), "s"(sw) : "memory"); break;
    }
  };
  int ob[4], wbb[2];
#pragma unroll
  for (int ks = 0; ks < 4; ++ks) ob[ks] = (96 * mw + r32) * 128 + ((2 * ks + h) ^ ((r32 >> 1) & 7)) * 16;
#pragma unroll
  for (int c = 0; c < 2; ++c) wbb[c] = 24576 + (8 * h + q4) * 256 + (((2 * nw + c) ^ q4) * 64) + 32 * blk + 8 * p4;
  f32x16 acc[2][3];
#pragma unroll
  for (int c = 0; c < 2; ++c)
#pragma unroll
    for (int m = 0; m < 3; ++m) { acc[c][m] = f32x16{}; asm volatile("" : "+a"(acc[c][m])); }
  bf16x8 of[2][3];
  s16x4 wl[2][2], wh[2][2];
#pragma unroll
  for (int j = 0; j < 10; ++j) issue1(0, 0, j);
#pragma unroll
  for (int j = 0; j < 10; ++j) issue1(1, 1, j);
  asm volatile("s_waitcnt vmcnt(10)" ::: "memory");
  __builtin_amdgcn_s_barrier();
  asm volatile("" ::: "memory");
#pragma unroll
  for (int m = 0; m < 3; ++m) of[0][m] = lds_b128(smem + ob[0] + m * 4096);
#pragma unroll
  for (int c = 0; c < 2; ++c) { wl[0][c] = lds_tr(smem + wbb[c]); wh[0][c] = lds_tr(smem + wbb[c] + 4 * 256); }
  int buf = 0;
  auto stage = [&](int st, auto doIssue_, auto hasNext_, auto last2_) {
    constexpr bool doIssue = decltype(doIssue_)::value, hasNext = decltype(hasNext_)::value, last2 = decltype(last2_)::value;
    const char* pb = smem + buf * K2_STAGE;
    const int bn = (buf == 2) ? 0 : buf + 1, bp = (buf == 0) ? 2 : buf - 1;
    const char* pn = smem + bn * K2_STAGE;
#pragma unroll
    for (int ks = 0; ks < 4; ++ks) {
      const int cu = ks & 1, nx = cu ^ 1;
      if (ks == 3) {
        if constexpr (hasNext) {
          if constexpr (last2) { asm volatile("s_waitcnt vmcnt(0) lgkmcnt(0)" ::: "memory"); } else { asm volatile("s_waitcnt vmcnt(10) lgkmcnt(0)" ::: "memory"); }
          __builtin_amdgcn_s_barrier();
          asm volatile("" ::: "memory");
        }
      }
      const bool ld = (ks < 3) || hasNext;
      const char* pr = (ks < 3) ? pb : pn;
      const int kn = (ks + 1) & 3;
#pragma unroll
      for (int gq = 0; gq < 6; ++gq) {
        const int c = gq / 3, m = gq % 3;
        mfma32_a(acc[c][m], cat8(wl[cu][c], wh[cu][c]), of[cu][m]);
        if (ld) {
          if (gq == 0) { wl[nx][0] = lds_tr(pr + wbb[0] + (16 * kn) * 256); wh[nx][0] = lds_tr(pr + wbb[0] + (16 * kn + 4) * 256); }
          else if (gq < 4) of[nx][gq - 1] = lds_b128(pr + ob[kn] + (gq - 1) * 4096);
          else if (gq == 4) { wl[nx][1] = lds_tr(pr + wbb[1] + (16 * kn) * 256); wh[nx][1] = lds_tr(pr + wbb[1] + (16 * kn + 4) * 256); }
        }
        if constexpr (doIssue) { if (ks < 3) { const int sl = ks * 6 + gq; if (sl % 5 != 4 && sl < 12) issue1(st + 2, bp, sl - sl / 5); } }
        SB();
      }
    }
    buf = bn;
  };
  constexpr std::true_type T_{}; constexpr std::false_type F_{};
  for (int st = 0; st < K2_NST - 2; ++st) stage(st, T_, T_, F_);
  stage(K2_NST - 2, F_, T_, T_);
  stage(K2_NST - 1, F_, F_, F_);
  MFMA_DRAIN();
  float* slab = part + (size_t)tl * (3 * 4 * 6144);
  const __amdgpu_buffer_rsrc_t rP = __builtin_amdgcn_make_buffer_rsrc((void*)slab, 0, 3 * 4 * 24576, 0x00020000);
#pragma unroll
  for (int c = 0; c < 2; ++c)
#pragma unroll
    for (int m = 0; m < 3; ++m) {
      if (m != sk) {
        asm volatile("" : "+a"(acc[c][m]));
#pragma unroll
        for (int i = 0; i < 4; ++i) {
          const f32x4 v = {acc[c][m][4 * i], acc[c][m][4 * i + 1], acc[c][m][4 * i + 2], acc[c][m][4 * i + 3]};
          __builtin_amdgcn_raw_buffer_store_b128(__builtin_bit_cast(u32x4, v), rP, l * 16, (unsigned)((sk * 4 + w) * 24576 + (c * 3 + m) * 4096 + i * 1024), 16);
        }
      }
    }
  asm volatile("s_waitcnt vmcnt(0)" ::: "memory");
  __syncthreads();
  if (tid == 0) __hip_atomic_fetch_add(cnt + tl * 32, 1u, __ATOMIC_RELAXED, __HIP_MEMORY_SCOPE_AGENT);
  if (w == 0) {
    unsigned spins = 0;
    while (__hip_atomic_load(cnt + tl * 32, __ATOMIC_RELAXED, __HIP_MEMORY_SCOPE_AGENT) < 3u) { __builtin_amdgcn_s_sleep(1); if (++spins > (1u << 24)) break; }
  }
  __syncthreads();
  auto fin = [&](auto J_) {
    constexpr int J = decltype(J_)::value;
    u32x4 v[2][2][4];
#pragma unroll
    for (int s2 = 0; s2 < 2; ++s2) {
      const int src = s2 + (s2 >= J ? 1 : 0);
#pragma unroll
      for (int c = 0; c < 2; ++c)
#pragma unroll
        for (int i = 0; i < 4; ++i)
          v[s2][c][i] = __builtin_amdgcn_raw_buffer_load_b128(rP, l * 16, (unsigned)((src * 4 + w) * 24576 + (c * 3 + J) * 4096 + i * 1024), 16);
    }
    u16* orow = out + (size_t)(mt * 192 + 96 * mw + 32 * J + r32) * (2 * DM) + br * DM + nt * 128 + 64 * nw + 8 * h;
#pragma unroll
    for (int c = 0; c < 2; ++c) {
      asm volatile("" : "+a"(acc[c][J]));
      unsigned pk[4][2];
#pragma unroll
      for (int i = 0; i < 4; ++i) {
        const f32x4 own = {acc[c][J][4 * i], acc[c][J][4 * i + 1], acc[c][J][4 * i + 2], acc[c][J][4 * i + 3]};
        f32x4 t[3];
#pragma unroll
        for (int s3 = 0; s3 < 3; ++s3) t[s3] = (s3 == J) ? own : __builtin_bit_cast(f32x4, v[s3 - (s3 > J ? 1 : 0)][c][i]);
        const f32x4 sum = (t[0] + t[1]) + t[2];
        pk[i][0] = cvtpk(sum[0], sum[1]); pk[i][1] = cvtpk(sum[2], sum[3]);
      }
#pragma unroll
      for (int i = 0; i < 4; i += 2) {
        auto r0 = __builtin_amdgcn_permlane32_swap(pk[i][0], pk[i + 1][0], false, false);
        auto r1 = __builtin_amdgcn_permlane32_swap(pk[i][1], pk[i + 1][1], false, false);
        u32x4 o = {r0[0], r1[0], r0[1], r1[1]};
        *reinterpret_cast<u32x4*>(orow + 32 * c + 8 * i) = o;
      }
    }
  };
  if (sk == 0) fin(std::integral_constant<int, 0>{});
  else if (sk == 1) fin(std::integral_constant<int, 1>{});
  else fin(std::integral_constant<int, 2>{});
}

extern "C" void kernel_launch(void* const* d_in, const int* in_sizes, int n_in,
                              void* d_out, int out_size, void* d_ws, size_t ws_size,
                              hipStream_t stream) {
  const u16* xA = (const u16*)d_in[0]; const u16* kA = (const u16*)d_in[1]; const u16* vA = (const u16*)d_in[2];
  const u16* wqA = (const u16*)d_in[3]; const u16* woA = (const u16*)d_in[4];
  const u16* xB = (const u16*)d_in[5]; const u16* kB = (const u16*)d_in[6]; const u16* vB = (const u16*)d_in[7];
  const u16* wqB = (const u16*)d_in[8]; const u16* woB = (const u16*)d_in[9];
  u16* Og = (u16*)d_ws;
  float* part = (float*)((char*)d_ws + WS_PART_OFF);
  unsigned* cnt = (unsigned*)((char*)d_ws + WS_CNT_OFF);
  (void)hipFuncSetAttribute((const void*)k1_attn, hipFuncAttributeMaxDynamicSharedMemorySize, K1_LDS);
  (void)hipFuncSetAttribute((const void*)k2_oproj, hipFuncAttributeMaxDynamicSharedMemorySize, K2_LDS);
  hipLaunchKernelGGL(k1_attn, dim3(256), dim3(256), K1_LDS, stream, xA, kA, vA, wqA, xB, kB, vB, wqB, Og, cnt);
  hipLaunchKernelGGL(k2_oproj, dim3(240), dim3(320), K2_LDS, stream, Og, woA, woB, (u16*)d_out, part, cnt);
}
```

```cpp
#include <hip/hip_runtime.h>
#include <hip/hip_bf16.h>
#include <stdint.h>
#include <type_traits>

#define DI __device__ __forceinline__
typedef unsigned short u16;
typedef __attribute__((ext_vector_type(8))) __bf16 bf16x8;
typedef __attribute__((ext_vector_type(2))) __bf16 bf16x2;
typedef __attribute__((ext_vector_type(4))) short s16x4;
typedef __attribute__((ext_vector_type(8))) short s16x8;
typedef __attribute__((ext_vector_type(4))) float f32x4;
typedef __attribute__((ext_vector_type(2))) float f32x2;
typedef __attribute__((ext_vector_type(4))) unsigned u32x4;
typedef __attribute__((address_space(3))) void lds_void;
typedef __attribute__((address_space(3))) s16x4 lds_s16x4;

constexpr int NB = 8, SQ = 192, DM = 640, NH = 32, SK = 512, DH = 192;
constexpr int HD = NH * DH;
constexpr int MROWS = NB * SQ;
constexpr int SLOT = 24576;
constexpr int K1_LDS = 160 * 1024;

DI unsigned cvtpk(float lo, float hi) {
  f32x2 v = {lo, hi};
  bf16x2 b = __builtin_convertvector(v, bf16x2);
  return __builtin_bit_cast(unsigned, b);
}
DI void mfma_a(f32x4& c, bf16x8 a, bf16x8 b) { asm volatile("v_mfma_f32_16x16x32_bf16 %0, %1, %2, %0" : "+a"(c) : "v"(a), "v"(b)); }
DI void mfma_v(f32x4& c, bf16x8 a, bf16x8 b) { asm volatile("v_mfma_f32_16x16x32_bf16 %0, %1, %2, %0" : "+v"(c) : "v"(a), "a"(b)); }
DI void mfma_a0(f32x4& c, bf16x8 a, bf16x8 b) { asm volatile("v_mfma_f32_16x16x32_bf16 %0, %1, %2, 0" : "=a"(c) : "v"(a), "v"(b)); }
DI void mfma_v0(f32x4& d, bf16x8 a, bf16x8 b, f32x4 c) { asm volatile("v_mfma_f32_16x16x32_bf16 %0, %1, %2, %3" : "=&v"(d) : "v"(a), "a"(b), "v"(c)); }
DI float fadd_s(float a, float b) { float r = a + b; asm("" : "+v"(r)); return r; }
#define MFMA_DRAIN() asm volatile("s_nop 7\n\ts_nop 7" ::: "memory")
DI bf16x8 lds_b128(const char* p) { return *reinterpret_cast<const bf16x8*>(p); }
DI s16x4 lds_tr(const char* p) { return __builtin_amdgcn_ds_read_tr16_b64_v4i16((lds_s16x4*)p); }
DI bf16x8 cat8(s16x4 lo, s16x4 hi) { s16x8 v = __builtin_shufflevector(lo, hi, 0, 1, 2, 3, 4, 5, 6, 7); return __builtin_bit_cast(bf16x8, v); }
#define SB() __builtin_amdgcn_sched_barrier(0)
#define PHASE_SYNC(n) do { asm volatile("s_waitcnt vmcnt(" #n ") lgkmcnt(0)" ::: "memory"); __builtin_amdgcn_s_barrier(); asm volatile("" ::: "memory"); } while (0)

DI u32x4 pair_fix(unsigned a0, unsigned a1, unsigned b0, unsigned b1) {
  auto r0 = __builtin_amdgcn_permlane32_swap(a0, b0, false, false);
  auto r1 = __builtin_amdgcn_permlane32_swap(a1, b1, false, false);
  auto s0 = __builtin_amdgcn_permlane16_swap(r0[0], r0[1], false, false);
  auto s1 = __builtin_amdgcn_permlane16_swap(r1[0], r1[1], false, false);
  u32x4 o = {s0[0], s1[0], s0[1], s1[1]};
  return o;
}

struct DmaOff { unsigned k[6], v[6], w[6], x[6]; };

struct DmaJob { __amdgpu_buffer_rsrc_t rs; unsigned soff, lb; unsigned vo[6]; };
DI DmaJob mkjob(__amdgpu_buffer_rsrc_t rs, const unsigned (&vo)[6], unsigned soff, char* smem, int slot, int w) {
  DmaJob j; j.rs = rs; j.soff = soff; j.lb = (unsigned)(size_t)smem + (unsigned)(slot * SLOT + w * 6144);
#pragma unroll
  for (int i = 0; i < 6; ++i) j.vo[i] = vo[i];
  return j;
}
struct DmaJobNT : DmaJob {};
DI DmaJobNT mkjob_nt(__amdgpu_buffer_rsrc_t rs, const unsigned (&vo)[6], unsigned soff, char* smem, int slot, int w) { DmaJobNT j; (DmaJob&)j = mkjob(rs, vo, soff, smem, slot, w); return j; }
template <int J, bool NT> DI void dma_piece_(const DmaJob& jb) {
  if constexpr ((J & 3) == 0) asm volatile("s_mov_b32 m0, %0\n\ts_nop 0" :: "s"(jb.lb + J * 1024u) : "memory");
  if constexpr (NT) asm volatile("buffer_load_dwordx4 %0, %1, %2 offen offset:%3 nt lds" :: "v"(jb.vo[J]), "s"(jb.rs), "s"(jb.soff), "i"((J & 3) * 1024) : "memory");
  else              asm volatile("buffer_load_dwordx4 %0, %1, %2 offen offset:%3 lds" :: "v"(jb.vo[J]), "s"(jb.rs), "s"(jb.soff), "i"((J & 3) * 1024) : "memory");
}
template <bool NT> DI void dma_piece_sw(const DmaJob& jb, int j) {
  switch (j) { case 0: dma_piece_<0, NT>(jb); break; case 1: dma_piece_<1, NT>(jb); break; case 2: dma_piece_<2, NT>(jb); break;
               case 3: dma_piece_<3, NT>(jb); break; case 4: dma_piece_<4, NT>(jb); break; default: dma_piece_<5, NT>(jb); break; }
}
DI void dma_piece(const DmaJobNT& jb, int j) { dma_piece_sw<true>(jb, j); }
DI void dma_piece(const DmaJob& jb, int j) { dma_piece_sw<false>(jb, j); }

__global__ __launch_bounds__(256, 1) void k1_attn(const u16* __restrict__ xA, const u16* __restrict__ kA, const u16* __restrict__ vA, const u16* __restrict__ wqA,
                                                  const u16* __restrict__ xB, const u16* __restrict__ kB, const u16* __restrict__ vB, const u16* __restrict__ wqB,
                                                  u16* __restrict__ Og, unsigned* __restrict__ cnt) {
  extern __shared__ __attribute__((aligned(16))) char smem[];
  const int tid = threadIdx.x, l = tid & 63;
  const int w = __builtin_amdgcn_readfirstlane(tid >> 6);
  const int r = l & 15, g = l >> 4, q4 = (l & 15) >> 2, p4 = l & 3;
  const int wg = blockIdx.x, xcd = wg & 7, idx = wg >> 3;
  const int br = xcd >> 2, hp = (xcd & 3) * 4 + (idx & 3), b = idx >> 2;

  if (blockIdx.x == 0 && tid < 80) cnt[tid * 32] = 0u;
  DmaOff D;
#pragma unroll
  for (int j = 0; j < 6; ++j) {
    const int p = (w * 6 + j) * 64 + l;
    const int row = p / 24, chp = p % 24;
    D.k[j] = row * 384 + ((chp & ~7) | ((chp & 7) ^ ((row >> 1) & 7))) * 16;
    D.v[j] = row * 384 + ((chp & ~7) | ((chp & 7) ^ (((row >> 1) & 3) << 1))) * 16;
    D.w[j] = row * 12288 + ((chp & ~7) | ((chp & 7) ^ ((((row >> 1) & 1) << 1) | (((row >> 3) & 1) << 2)))) * 16;
    const int rx = p >> 3, cx = p & 7;
    D.x[j] = rx * 1280 + (cx ^ ((rx >> 1) & 7)) * 16;
    const unsigned bias = 3072u - 1024u * (j & 3);
    D.k[j] += bias; D.v[j] += bias; D.w[j] += bias; D.x[j] += bias;
  }
  const int kb0 = r * 384 + ((g) ^ (r >> 1)) * 16;
  const int kb1 = r * 384 + ((4 + g) ^ (r >> 1)) * 16;
  int vbs[4], wbs[4];
  {
    const int fv = ((2 * g + (q4 >> 1)) & 3) << 1;
    const int fw = ((q4 >> 1) << 1) | ((g & 1) << 2);
#pragma unroll
    for (int c = 0; c < 4; ++c) {
      vbs[c] = (4 * g + q4) * 384 + (((2 * c) ^ fv) + (p4 >> 1)) * 16 + 8 * (p4 & 1);
      wbs[c] = (8 * g + q4) * 384 + (((2 * c) ^ fw) + (p4 >> 1)) * 16 + 8 * (p4 & 1);
    }
  }
  const int xb0 = (48 * w + r) * 128 + ((g) ^ (r >> 1)) * 16;
  const int xb1 = (48 * w + r) * 128 + ((4 + g) ^ (r >> 1)) * 16;

  const unsigned xoff = (unsigned)b * (SQ * DM * 2);
  const unsigned woff = (unsigned)hp * (2 * DH * 2);
  const unsigned kvoff = (unsigned)(b * NH + 2 * hp) * (SK * DH * 2);

  const __amdgpu_buffer_rsrc_t rX = __builtin_amdgcn_make_buffer_rsrc((void*)((const char*)(br ? xB : xA) - 3072), 0, NB * SQ * DM * 2 + 3072, 0x00020000);
  const __amdgpu_buffer_rsrc_t rW = __builtin_amdgcn_make_buffer_rsrc((void*)((const char*)(br ? wqB : wqA) - 3072), 0, DM * HD * 2 + 3072, 0x00020000);
  const __amdgpu_buffer_rsrc_t rK = __builtin_amdgcn_make_buffer_rsrc((void*)((const char*)(br ? kB : kA) - 3072), 0, NB * NH * SK * DH * 2 + 3072, 0x00020000);
  const __amdgpu_buffer_rsrc_t rV = __builtin_amdgcn_make_buffer_rsrc((void*)((const char*)(br ? vB : vA) - 3072), 0, NB * NH * SK * DH * 2 + 3072, 0x00020000);

#define JOB_X(i, slot) mkjob(rX, D.x, xoff + (i) * 128u, smem, slot, w)
#define JOB_W(hh, i, slot) mkjob(rW, D.w, woff + (hh) * 384u + (i) * (64u * 12288u), smem, slot, w)
#define JOB_K(t, slot) mkjob_nt(rK, D.k, kvoff + (t) * 24576u, smem, slot, w)
#define JOB_V(t, slot) mkjob_nt(rV, D.v, kvoff + (t) * 24576u, smem, slot, w)
#define ISSUE(job) do { const DmaJob j_ = (job); _Pragma("unroll") for (int q_ = 0; q_ < 6; ++q_) dma_piece(j_, q_); } while (0)
  const DmaJob DUMMY = JOB_X(0, 0);

  ISSUE(JOB_X(0, 0)); ISSUE(JOB_W(0, 0, 1)); ISSUE(JOB_X(1, 2)); ISSUE(JOB_W(0, 1, 3)); ISSUE(JOB_X(2, 4)); ISSUE(JOB_W(0, 2, 5));

  const float QSCALE = 0.07216878364870322f * 1.4426950408889634f;
  constexpr std::true_type T_{}; constexpr std::false_type F_{};
  int ax = 0, aw = 1, bx = 2, bw = 3, cx = 4, cw = 5;

  for (int unit = 0; unit < 2; ++unit) {
    f32x4 acc[12][3];
    bf16x8 qf[3][6];
    {
      bf16x8 xf[2][3];
      s16x4 wlo[4], whi[4];
      auto waddr = [&](const char* pw, int i) { const int ks1 = i / 12, c1 = i % 12; return pw + wbs[c1 & 3] + (32 * ks1) * 384 + (c1 >> 2) * 128; };
      auto qphase = [&](int sx, int sw, int sxn, int swn, auto nj_, const auto& j0, const auto& j1, auto hasNext_, auto first_) {
        constexpr bool hasNext = decltype(hasNext_)::value, first = decltype(first_)::value;
        constexpr int nj = decltype(nj_)::value;
        const char* px = smem + sx * SLOT; const char* pw = smem + sw * SLOT;
        const char* pxn = smem + sxn * SLOT; const char* pwn = smem + swn * SLOT;
#pragma unroll
        for (int i = 0; i < 24; ++i) {
          const int ks = i / 12, c = i % 12;
          const bf16x8 wf = cat8(wlo[i & 3], whi[i & 3]);
          if constexpr (hasNext) { if (i == 21) { PHASE_SYNC(12); } }
          const bool ld = (i + 3 < 24) || hasNext;
          const char* an = (i + 3 < 24) ? waddr(pw, i + 3) : waddr(pwn, i + 3 - 24);
          if (first && ks == 0) mfma_a0(acc[c][0], wf, xf[ks][0]); else mfma_a(acc[c][0], wf, xf[ks][0]);
          if (ld) wlo[(i + 3) & 3] = lds_tr(an);
          SB();
          if (first && ks == 0) mfma_a0(acc[c][1], wf, xf[ks][1]); else mfma_a(acc[c][1], wf, xf[ks][1]);
          if (ld) whi[(i + 3) & 3] = lds_tr(an + 4 * 384);
          SB();
          if (first && ks == 0) mfma_a0(acc[c][2], wf, xf[ks][2]); else mfma_a(acc[c][2], wf, xf[ks][2]);
          if (i < 3) xf[1][i] = lds_b128(px + xb1 + i * 2048);
          else if (i >= 21) { if constexpr (hasNext) xf[0][i - 21] = lds_b128(pxn + xb0 + (i - 21) * 2048); }
          else if (i % 3 != 2) { const int q = ((i - 3) / 3) * 2 + (i % 3); if (q < 6) { if constexpr (nj > 0) dma_piece(j0, q); } else { if constexpr (nj > 1) dma_piece(j1, q - 6); } }
          SB();
        }
      };
      constexpr std::integral_constant<int, 1> J1{}; constexpr std::integral_constant<int, 2> J2{};
      if (unit == 0) { PHASE_SYNC(24); } else { PHASE_SYNC(36); }
#pragma unroll
      for (int i = 0; i < 3; ++i) xf[0][i] = lds_b128(smem + ax * SLOT + xb0 + i * 2048);
#pragma unroll
      for (int i = 0; i < 3; ++i) { const char* a = waddr(smem + aw * SLOT, i); wlo[i] = lds_tr(a); whi[i] = lds_tr(a + 4 * 384); }
      qphase(ax, aw, bx, bw, J1, JOB_W(unit, 2, cw), DUMMY, T_, T_);
      for (int i = 1; i < 8; ++i) {
        qphase(bx, bw, cx, cw, J2, JOB_X(i + 2, ax), JOB_W(unit, i + 2, aw), T_, F_);
        { const int tx = ax, tw = aw; ax = bx; aw = bw; bx = cx; bw = cw; cx = tx; cw = tw; }
      }
      qphase(bx, bw, cx, cw, J2, JOB_K(8 * unit, ax), JOB_K(8 * unit + 1, aw), T_, F_);
      qphase(cx, cw, cx, cw, J2, JOB_K(8 * unit + 2, bx), JOB_V(8 * unit, bw), F_, F_);

      MFMA_DRAIN();
#pragma unroll
      for (int qt = 0; qt < 3; ++qt)
#pragma unroll
        for (int s = 0; s < 6; ++s) {
          asm volatile("" : "+a"(acc[2 * s][qt]), "+a"(acc[2 * s + 1][qt]));
          const f32x4 A = acc[2 * s][qt] * QSCALE, B = acc[2 * s + 1][qt] * QSCALE;
          u32x4 o = pair_fix(cvtpk(A[0], A[1]), cvtpk(A[2], A[3]), cvtpk(B[0], B[1]), cvtpk(B[2], B[3]));
          qf[qt][s] = __builtin_bit_cast(bf16x8, o);
          asm volatile("" : "+a"(qf[qt][s]));
          if (s % 3 == 2) SB();
        }
    }

    float mref[3], lsum[3] = {0.f, 0.f, 0.f};
    f32x4 negm[3] = {f32x4{0.f, 0.f, 0.f, 0.f}, f32x4{0.f, 0.f, 0.f, 0.f}, f32x4{0.f, 0.f, 0.f, 0.f}};
    asm volatile("s_nop 1" : "+v"(negm[0]), "+v"(negm[1]), "+v"(negm[2]));
    f32x4 Sc[2][3];
    bf16x8 Pp[3];
    bf16x8 kf[3];
    s16x4 vlo[3], vhi[3];

    auto sm = [&](const f32x4 (&S)[2][3], bf16x8 (&P)[3], float (&ps)[3]) {
#pragma unroll
      for (int qt = 0; qt < 3; ++qt) {
        float e[8];
#pragma unroll
        for (int i = 0; i < 8; ++i) e[i] = __builtin_amdgcn_exp2f(S[i >> 2][qt][i & 3]);
        ps[qt] = ((e[0] + e[1]) + (e[2] + e[3])) + ((e[4] + e[5]) + (e[6] + e[7]));
        u32x4 o = {cvtpk(e[0], e[1]), cvtpk(e[2], e[3]), cvtpk(e[4], e[5]), cvtpk(e[6], e[7])};
        P[qt] = __builtin_bit_cast(bf16x8, o);
      }
    };
    auto rowmax = [&](const f32x4 (&S)[2][3], int qt) {
      float mx = fmaxf(fmaxf(fmaxf(S[0][qt][0], S[0][qt][1]), fmaxf(S[0][qt][2], S[0][qt][3])),
                       fmaxf(fmaxf(S[1][qt][0], S[1][qt][1]), fmaxf(S[1][qt][2], S[1][qt][3])));
      mx = fmaxf(mx, __shfl_xor(mx, 16));
      mx = fmaxf(mx, __shfl_xor(mx, 32));
      return mx;
    };
    auto kaddr = [&](const char* pk, int i) { const int s1 = i >> 1, k21 = i & 1; return pk + ((s1 & 1) ? kb1 : kb0) + k21 * 6144 + (s1 >> 1) * 128; };
    auto vaddr = [&](const char* pv, int c1) { return pv + vbs[c1 & 3] + (c1 >> 2) * 128; };
    auto kbase = [&](int slot, int hb) { return (const char*)smem + slot * SLOT + hb * (2 * 6144); };
    auto vbase = [&](int slot, int ks) { return (const char*)smem + slot * SLOT + (32 * ks) * 384; };
    auto step = [&](auto doQK_, const char* pk, auto doPV_, const char* pvv, auto doSM_, auto hasJob_, const auto& jb,
                    auto nQK_, const char* pkn, auto nPV_, const char* pvn, auto hasSync_, auto zeroO_) {
      constexpr bool zeroO = decltype(zeroO_)::value;
      constexpr bool hasJob = decltype(hasJob_)::value;
      constexpr bool doQK = decltype(doQK_)::value, doPV = decltype(doPV_)::value, doSM = decltype(doSM_)::value;
      constexpr bool nQK = decltype(nQK_)::value, nPV = decltype(nPV_)::value, hasSync = decltype(hasSync_)::value;
      f32x4 Sn[2][3];
      u32x4 Pn[3];
      float ps[3] = {0.f, 0.f, 0.f};
      float ea = 0.f, eb = 0.f;
#pragma unroll
      for (int i = 0; i < 12; ++i) {
        const int s = i >> 1, k2 = i & 1, ri = i % 3, rn = (i + 2) % 3;
        if constexpr (hasSync) { if (i == 10) { PHASE_SYNC(12); } }
        const bool in = (i + 2 < 12);
        const bool ldK = in ? doQK : nQK, ldV = in ? doPV : nPV;
        const char* ka = in ? kaddr(pk, i + 2) : kaddr(pkn, i + 2 - 12);
        const char* va = in ? vaddr(pvv, i + 2) : vaddr(pvn, i + 2 - 12);
        const bf16x8 kc = kf[ri];
        const bf16x8 vc = cat8(vlo[ri], vhi[ri]);
        const int pq = (i - 1) >> 2, pj = (i - 1) & 3;
        const int eq = i >> 2, e0 = (i & 3) * 2;
        if constexpr (doQK) { if (s == 0) mfma_v0(Sn[k2][0], kc, qf[0][s], negm[0]); else mfma_v(Sn[k2][0], kc, qf[0][s]); }
        if (ldV) vlo[rn] = lds_tr(va);
        if constexpr (doSM) { if (i > 0) { unsigned pk2 = cvtpk(ea, eb); asm volatile("" : "+v"(pk2)); Pn[pq][pj] = pk2; } }
        SB();
        if constexpr (doQK) { if (s == 0) mfma_v0(Sn[k2][1], kc, qf[1][s], negm[1]); else mfma_v(Sn[k2][1], kc, qf[1][s]); }
        if (ldV) vhi[rn] = lds_tr(va + 16 * 384);
        if constexpr (doSM) { if (i > 0) { if (pj == 0) ps[pq] = fadd_s(ea, eb); else ps[pq] = fadd_s(ps[pq], ea); } }
        SB();
        if constexpr (doQK) { if (s == 0) mfma_v0(Sn[k2][2], kc, qf[2][s], negm[2]); else mfma_v(Sn[k2][2], kc, qf[2][s]); }
        if (ldK) kf[rn] = lds_b128(ka);
        if constexpr (doSM) { if (i > 0) { if (pj != 0) ps[pq] = fadd_s(ps[pq], eb); } }
        SB();
        if constexpr (doPV) { if constexpr (zeroO) mfma_a0(acc[i][0], vc, Pp[0]); else mfma_a(acc[i][0], vc, Pp[0]); }
        if constexpr (doSM) { ea = __builtin_amdgcn_exp2f(Sc[e0 >> 2][eq][e0 & 3]); asm volatile("" : "+v"(ea)); }
        SB();
        if constexpr (doPV) { if constexpr (zeroO) mfma_a0(acc[i][1], vc, Pp[1]); else mfma_a(acc[i][1], vc, Pp[1]); }
        if constexpr (doSM) { eb = __builtin_amdgcn_exp2f(Sc[e0 >> 2][eq][(e0 & 3) + 1]); asm volatile("" : "+v"(eb)); }
        SB();
        if constexpr (doPV) { if constexpr (zeroO) mfma_a0(acc[i][2], vc, Pp[2]); else mfma_a(acc[i][2], vc, Pp[2]); }
        if constexpr (hasJob) { if (i < 10) { if ((i & 1) == 0) dma_piece(jb, i >> 1); else if (i == 9) dma_piece(jb, 5); } }
        if constexpr (doQK) { if (i == 2) asm volatile("" :: "v"(negm[0]), "v"(negm[1]), "v"(negm[2])); }
        SB();
      }
      if constexpr (doSM) {
        Pn[2][3] = cvtpk(ea, eb);
        ps[2] = fadd_s(ps[2], ea); ps[2] = fadd_s(ps[2], eb);
        const bool bad = !(ps[0] <= 1.0e12f) || !(ps[1] <= 1.0e12f) || !(ps[2] <= 1.0e12f);
        bf16x8 Pb[3];
#pragma unroll
        for (int qt = 0; qt < 3; ++qt) Pb[qt] = __builtin_bit_cast(bf16x8, Pn[qt]);
        if (__builtin_expect(__any(bad), 0)) {
          MFMA_DRAIN();
          if constexpr (doQK) asm volatile("" : "+v"(Sn[0][0]), "+v"(Sn[0][1]), "+v"(Sn[0][2]), "+v"(Sn[1][0]), "+v"(Sn[1][1]), "+v"(Sn[1][2]));
#pragma unroll
          for (int qt = 0; qt < 3; ++qt) {
            const float d = fmaxf(rowmax(Sc, qt), 0.f);
            const float f = __builtin_amdgcn_exp2f(-d);
            mref[qt] += d;
            negm[qt] = f32x4{-mref[qt], -mref[qt], -mref[qt], -mref[qt]};
            asm volatile("s_nop 1" : "+v"(negm[qt]));
            lsum[qt] *= f;
#pragma unroll
            for (int c = 0; c < 12; ++c) { asm volatile("" : "+a"(acc[c][qt])); acc[c][qt] *= f; asm volatile("" : "+a"(acc[c][qt])); SB(); }
#pragma unroll
            for (int k2 = 0; k2 < 2; ++k2) { Sc[k2][qt] -= d; if constexpr (doQK) Sn[k2][qt] -= d; }
          }
          sm(Sc, Pb, ps);
        }
#pragma unroll
        for (int qt = 0; qt < 3; ++qt) { lsum[qt] += ps[qt]; Pp[qt] = Pb[qt]; asm volatile("s_nop 0" : "+v"(Pp[qt])); }
      }
      if constexpr (doQK) {
#pragma unroll
        for (int qt = 0; qt < 3; ++qt) { Sc[0][qt] = Sn[0][qt]; Sc[1][qt] = Sn[1][qt]; }
      }
    };

    const int G0 = unit * 8;
    int kc = ax, kn = aw, kp = bx;
    int vn = bw, vp = cx, vc = cw;
#define ROTK() do { const int x_ = kp; kp = kc; kc = kn; kn = x_; } while (0)
#define ROTV() do { const int x_ = vp; vp = vc; vc = vn; vn = x_; } while (0)
    PHASE_SYNC(18);
    {
      kf[0] = lds_b128(kaddr(kbase(kc, 0), 0)); kf[1] = lds_b128(kaddr(kbase(kc, 0), 1));
      step(T_, kbase(kc, 0), F_, smem, F_, T_, JOB_V(G0 + 1, vp), T_, kbase(kc, 1), F_, smem, F_, F_);
      asm volatile("s_nop 7\n\ts_nop 7" : "+v"(Sc[0][0]), "+v"(Sc[0][1]), "+v"(Sc[0][2]), "+v"(Sc[1][0]), "+v"(Sc[1][1]), "+v"(Sc[1][2]));
#pragma unroll
      for (int qt = 0; qt < 3; ++qt) {
        const float mx = rowmax(Sc, qt);
        mref[qt] = mx;
        negm[qt] = f32x4{-mx, -mx, -mx, -mx};
        asm volatile("s_nop 1" : "+v"(negm[qt]));
        Sc[0][qt] -= mx; Sc[1][qt] -= mx;
      }
      step(T_, kbase(kc, 1), F_, smem, T_, T_, JOB_V(G0 + 2, vc), T_, kbase(kn, 0), T_, vbase(vn, 0), T_, F_);
      ROTK(); ROTV();
      step(T_, kbase(kc, 0), T_, vbase(vc, 0), T_, T_, JOB_K(G0 + 3, kp), T_, kbase(kc, 1), T_, vbase(vc, 1), F_, T_);
      step(T_, kbase(kc, 1), T_, vbase(vc, 1), T_, F_, DUMMY, T_, kbase(kn, 0), T_, vbase(vn, 0), T_, F_);
      ROTK(); ROTV();
    }
    for (int t = 2; t < 6; ++t) {
      step(T_, kbase(kc, 0), T_, vbase(vc, 0), T_, T_, JOB_K(G0 + t + 2, kp), T_, kbase(kc, 1), T_, vbase(vc, 1), F_, F_);
      step(T_, kbase(kc, 1), T_, vbase(vc, 1), T_, T_, JOB_V(G0 + t + 1, vp), T_, kbase(kn, 0), T_, vbase(vn, 0), T_, F_);
      ROTK(); ROTV();
    }
    {
      ax = kp;
      step(T_, kbase(kc, 0), T_, vbase(vc, 0), T_, T_, JOB_X(0, ax), T_, kbase(kc, 1), T_, vbase(vc, 1), F_, F_);
      step(T_, kbase(kc, 1), T_, vbase(vc, 1), T_, T_, JOB_V(G0 + 7, vp), T_, kbase(kn, 0), T_, vbase(vn, 0), T_, F_);
      ROTK(); ROTV();
      aw = kp; bx = vp;
      step(T_, kbase(kc, 0), T_, vbase(vc, 0), T_, T_, JOB_W(1, 0, aw), T_, kbase(kc, 1), T_, vbase(vc, 1), F_, F_);
      step(T_, kbase(kc, 1), T_, vbase(vc, 1), T_, T_, JOB_X(1, bx), F_, smem, T_, vbase(vn, 0), T_, F_);
      ROTK(); ROTV();
      bw = kp; cx = vp; cw = vc;
      step(F_, smem, T_, vbase(vc, 0), T_, T_, JOB_W(1, 1, bw), F_, smem, T_, vbase(vc, 1), F_, F_);
      step(F_, smem, T_, vbase(vc, 1), F_, T_, JOB_X(2, cx), F_, smem, F_, smem, F_, F_);
    }

    MFMA_DRAIN();
    {
      u16* orow = Og + (size_t)br * MROWS * HD + ((size_t)(3 * (2 * hp + unit)) * MROWS + (b * SQ + 48 * w + r)) * 64 + 8 * g;
#pragma unroll
      for (int qt = 0; qt < 3; ++qt) {
        float lt = lsum[qt];
        lt += __shfl_xor(lt, 16);
        lt += __shfl_xor(lt, 32);
        const float inv = 1.0f / lt;
#pragma unroll
        for (int s = 0; s < 6; ++s) {
          asm volatile("" : "+a"(acc[2 * s][qt]), "+a"(acc[2 * s + 1][qt]));
          const f32x4 A = acc[2 * s][qt] * inv, B = acc[2 * s + 1][qt] * inv;
          u32x4 o = pair_fix(cvtpk(A[0], A[1]), cvtpk(A[2], A[3]), cvtpk(B[0], B[1]), cvtpk(B[2], B[3]));
          *reinterpret_cast<u32x4*>(orow + (size_t)(s >> 1) * MROWS * 64 + qt * 16 * 64 + 32 * (s & 1)) = o;
          if (s % 3 == 2) SB();
        }
      }
    }
  }
  asm volatile("s_waitcnt vmcnt(0)" ::: "memory");
}

typedef __attribute__((ext_vector_type(16))) float f32x16;
constexpr int K2_STAGE = 24576 + 16384;
constexpr int K2_LDS = 3 * K2_STAGE + 256;
constexpr int K2_NST = 32;
#define K2_PF 8
constexpr size_t WS_O_BYTES = (size_t)2 * MROWS * HD * 2;
constexpr size_t WS_PART_OFF = WS_O_BYTES;
constexpr size_t WS_PART_BYTES = (size_t)80 * 3 * 4 * 24576;
constexpr size_t WS_CNT_OFF = WS_PART_OFF + WS_PART_BYTES;
DI void mfma32_a(f32x16& c, bf16x8 a, bf16x8 b) { asm volatile("v_mfma_f32_32x32x16_bf16 %0, %1, %2, %0" : "+a"(c) : "v"(a), "v"(b)); }

__global__ __launch_bounds__(320, 1) void k2_oproj(const u16* __restrict__ Og, const u16* __restrict__ woA, const u16* __restrict__ woB,
                                                   u16* __restrict__ out, float* __restrict__ part, unsigned* __restrict__ cnt) {
  extern __shared__ __attribute__((aligned(16))) char smem[];
  const int tid = threadIdx.x, l = tid & 63;
  const int w = __builtin_amdgcn_readfirstlane(tid >> 6);
  const int r32 = l & 31, h = l >> 5, blk = (l >> 4) & 1, q4 = (l & 15) >> 2, p4 = l & 3;
  const int mw = (w >> 1) & 1, nw = w & 1;
  const int wg = blockIdx.x, item = (wg & 7) * 30 + (wg >> 3);
  const int br = item / 120, sk = (item / 40) % 3, mt = (item % 40) / 5, nt = item % 5;
  const int tl = br * 40 + mt * 5 + nt;
  const u16* wo = br ? woB : woA;
  const __amdgpu_buffer_rsrc_t rO = __builtin_amdgcn_make_buffer_rsrc((void*)(Og + (size_t)br * MROWS * HD), 0, MROWS * HD * 2, 0x00020000);
  const __amdgpu_buffer_rsrc_t rW = __builtin_amdgcn_make_buffer_rsrc((void*)wo, 0, HD * DM * 2, 0x00020000);
  const unsigned ooff = ((unsigned)(sk * K2_NST) * MROWS + (unsigned)(mt * 192)) * 128u;
  const unsigned woff = (unsigned)(sk * 2048) * (DM * 2) + (unsigned)nt * 256;
  if (w == 4) {
    const unsigned to = (l < 39 && nt * 39 + l < 192) ? (unsigned)(nt * 39 + l) * 128u : 0x7fffffffu;
    const unsigned tw = (l < 16) ? (unsigned)(mt * 8 + (l >> 1)) * (DM * 2) + (unsigned)(l & 1) * 128u : 0x7fffffffu;
    const unsigned tlb = (unsigned)(size_t)smem + 3u * K2_STAGE;
    auto tch = [&](int st) {
      asm volatile("s_mov_b32 m0, %0\n\ts_nop 0\n\tbuffer_load_dword %1, %2, %3 offen lds" :: "s"(tlb), "v"(to), "s"(rO), "s"(ooff + st * (MROWS * 128u)) : "memory");
      asm volatile("buffer_load_dword %0, %1, %2 offen lds" :: "v"(tw), "s"(rW), "s"(woff + st * (64u * DM * 2)) : "memory");
    };
#pragma unroll
    for (int st = 2; st < 2 + K2_PF; ++st) tch(st);
    __builtin_amdgcn_s_barrier();
    for (int st = 0; st < K2_NST - 1; ++st) {
      if (st + 2 + K2_PF < K2_NST) tch(st + 2 + K2_PF);
      __builtin_amdgcn_s_barrier();
    }
    asm volatile("s_waitcnt vmcnt(0)" ::: "memory");
    return;
  }
  unsigned vo[6], vw[4];
#pragma unroll
  for (int j = 0; j < 6; ++j) { const int p = (w * 6 + j) * 64 + l, rx = p >> 3, cx = p & 7; vo[j] = rx * 128 + (cx ^ ((rx >> 1) & 7)) * 16 + 3072u - 1024u * (j & 3); }
#pragma unroll
  for (int j = 0; j < 4; ++j) { const int p = (w * 4 + j) * 64 + l, row = p >> 4, ch = p & 15; vw[j] = row * (DM * 2) + (ch ^ ((row & 3) << 2)) * 16 + 3072u - 1024u * (j & 3); }
  const unsigned lbase = (unsigned)(size_t)smem;
  const __amdgpu_buffer_rsrc_t rOd = __builtin_amdgcn_make_buffer_rsrc((void*)((const char*)(Og + (size_t)br * MROWS * HD) - 3072), 0, MROWS * HD * 2 + 3072, 0x00020000);
  const __amdgpu_buffer_rsrc_t rWd = __builtin_amdgcn_make_buffer_rsrc((void*)((const char*)wo - 3072), 0, HD * DM * 2 + 3072, 0x00020000);
  auto issue1 = [&](int st, int buf, int j) {
    const unsigned lb = lbase + buf * K2_STAGE;
    if (j == 0 || j == 4) asm volatile("s_mov_b32 m0, %0\n\ts_nop 0" :: "s"(lb + (w * 6 + j) * 1024u) : "memory");
    if (j == 6) asm volatile("s_mov_b32 m0, %0\n\ts_nop 0" :: "s"(lb + 24576u + (w * 4) * 1024u) : "memory");
    const unsigned so = ooff + st * (MROWS * 128u), sw = woff + st * (64u * DM * 2);
    switch (j) {
      case 0: asm volatile("buffer_load_dwordx4 %0, %1, %2 offen lds" :: "v"(vo[0]), "s"(rOd), "s"(so) : "memory"); break;
      case 1: asm volatile("buffer_load_dwordx4 %0, %1, %2 offen offset:1024 lds" :: "v"(vo[1]), "s"(rOd), "s"(so) : "memory"); break;
      case 2: asm volatile("buffer_load_dwordx4 %0, %1, %2 offen offset:2048 lds" :: "v"(vo[2]), "s"(rOd), "s"(so) : "memory"); break;
      case 3: asm volatile("buffer_load_dwordx4 %0, %1, %2 offen offset:3072 lds" :: "v"(vo[3]), "s"(rOd), "s"(so) : "memory"); break;
      case 4: asm volatile("buffer_load_dwordx4 %0, %1, %2 offen lds" :: "v"(vo[4]), "s"(rOd), "s"(so) : "memory"); break;
      case 5: asm volatile("buffer_load_dwordx4 %0, %1, %2 offen offset:1024 lds" :: "v"(vo[5]), "s"(rOd), "s"(so) : "memory"); break;
      case 6: asm volatile("buffer_load_dwordx4 %0, %1, %2 offen lds" :: "v"(vw[0]), "s"(rWd), "s"(sw) : "memory"); break;
      case 7: asm volatile("buffer_load_dwordx4 %0, %1, %2 offen offset:1024 lds" :: "v"(vw[1]), "s"(rWd), "s"(sw) : "memory"); break;
      case 8: asm volatile("buffer_load_dwordx4 %0, %1, %2 offen offset:2048 lds" :: "v"(vw[2]), "s"(rWd), "s"(sw) : "memory"); break;
      default: asm volatile("buffer_load_dwordx4 %0, %1, %2 offen offset:3072 lds" :: "v"(vw[3]), "s"(rWd), "s"(sw) : "memory"); break;
    }
  };
  int ob[4], wbb[2];
#pragma unroll
  for (int ks = 0; ks < 4; ++ks) ob[ks] = (96 * mw + r32) * 128 + ((2 * ks + h) ^ ((r32 >> 1) & 7)) * 16;
#pragma unroll
  for (int c = 0; c < 2; ++c) wbb[c] = 24576 + (8 * h + q4) * 256 + (((2 * nw + c) ^ q4) * 64) + 32 * blk + 8 * p4;
  f32x16 acc[2][3];
#pragma unroll
  for (int c = 0; c < 2; ++c)
#pragma unroll
    for (int m = 0; m < 3; ++m) { acc[c][m] = f32x16{}; asm volatile("" : "+a"(acc[c][m])); }
  bf16x8 of[2][3];
  s16x4 wl[2][2], wh[2][2];
#pragma unroll
  for (int j = 0; j < 10; ++j) issue1(0, 0, j);
#pragma unroll
  for (int j = 0; j < 10; ++j) issue1(1, 1, j);
  asm volatile("s_waitcnt vmcnt(10)" ::: "memory");
  __builtin_amdgcn_s_barrier();
  asm volatile("" ::: "memory");
#pragma unroll
  for (int m = 0; m < 3; ++m) of[0][m] = lds_b128(smem + ob[0] + m * 4096);
#pragma unroll
  for (int c = 0; c < 2; ++c) { wl[0][c] = lds_tr(smem + wbb[c]); wh[0][c] = lds_tr(smem + wbb[c] + 4 * 256); }
  int buf = 0;
  auto stage = [&](int st, auto doIssue_, auto hasNext_, auto last2_) {
    constexpr bool doIssue = decltype(doIssue_)::value, hasNext = decltype(hasNext_)::value, last2 = decltype(last2_)::value;
    const char* pb = smem + buf * K2_STAGE;
    const int bn = (buf == 2) ? 0 : buf + 1, bp = (buf == 0) ? 2 : buf - 1;
    const char* pn = smem + bn * K2_STAGE;
#pragma unroll
    for (int ks = 0; ks < 4; ++ks) {
      const int cu = ks & 1, nx = cu ^ 1;
      if (ks == 3) {
        if constexpr (hasNext) {
          if constexpr (last2) { asm volatile("s_waitcnt vmcnt(0) lgkmcnt(0)" ::: "memory"); } else { asm volatile("s_waitcnt vmcnt(10) lgkmcnt(0)" ::: "memory"); }
          __builtin_amdgcn_s_barrier();
          asm volatile("" ::: "memory");
        }
      }
      const bool ld = (ks < 3) || hasNext;
      const char* pr = (ks < 3) ? pb : pn;
      const int kn = (ks + 1) & 3;
#pragma unroll
      for (int gq = 0; gq < 6; ++gq) {
        const int c = gq / 3, m = gq % 3;
        mfma32_a(acc[c][m], cat8(wl[cu][c], wh[cu][c]), of[cu][m]);
        if (ld) {
          if (gq == 0) { wl[nx][0] = lds_tr(pr + wbb[0] + (16 * kn) * 256); wh[nx][0] = lds_tr(pr + wbb[0] + (16 * kn + 4) * 256); }
          else if (gq < 4) of[nx][gq - 1] = lds_b128(pr + ob[kn] + (gq - 1) * 4096);
          else if (gq == 4) { wl[nx][1] = lds_tr(pr + wbb[1] + (16 * kn) * 256); wh[nx][1] = lds_tr(pr + wbb[1] + (16 * kn + 4) * 256); }
        }
        if constexpr (doIssue) { if (ks < 3) { const int sl = ks * 6 + gq; if (sl % 5 != 4 && sl < 12) issue1(st + 2, bp, sl - sl / 5); } }
        SB();
      }
    }
    buf = bn;
  };
  constexpr std::true_type T_{}; constexpr std::false_type F_{};
  for (int st = 0; st < K2_NST - 2; ++st) stage(st, T_, T_, F_);
  stage(K2_NST - 2, F_, T_, T_);
  stage(K2_NST - 1, F_, F_, F_);
  MFMA_DRAIN();
  float* slab = part + (size_t)tl * (3 * 4 * 6144);
  const __amdgpu_buffer_rsrc_t rP = __builtin_amdgcn_make_buffer_rsrc((void*)slab, 0, 3 * 4 * 24576, 0x00020000);
#pragma unroll
  for (int c = 0; c < 2; ++c)
#pragma unroll
    for (int m = 0; m < 3; ++m) {
      if (m != sk) {
        asm volatile("" : "+a"(acc[c][m]));
#pragma unroll
        for (int i = 0; i < 4; ++i) {
          const f32x4 v = {acc[c][m][4 * i], acc[c][m][4 * i + 1], acc[c][m][4 * i + 2], acc[c][m][4 * i + 3]};
          __builtin_amdgcn_raw_buffer_store_b128(__builtin_bit_cast(u32x4, v), rP, l * 16, (unsigned)((sk * 4 + w) * 24576 + (c * 3 + m) * 4096 + i * 1024), 16);
        }
      }
    }
  asm volatile("s_waitcnt vmcnt(0)" ::: "memory");
  __syncthreads();
  if (tid == 0) __hip_atomic_fetch_add(cnt + tl * 32, 1u, __ATOMIC_RELAXED, __HIP_MEMORY_SCOPE_AGENT);
  if (w == 0) {
    unsigned spins = 0;
    while (__hip_atomic_load(cnt + tl * 32, __ATOMIC_RELAXED, __HIP_MEMORY_SCOPE_AGENT) < 3u) { __builtin_amdgcn_s_sleep(1); if (++spins > (1u << 24)) break; }
  }
  __syncthreads();
  auto fin = [&](auto J_) {
    constexpr int J = decltype(J_)::value;
    u32x4 v[2][2][4];
#pragma unroll
    for (int s2 = 0; s2 < 2; ++s2) {
      const int src = s2 + (s2 >= J ? 1 : 0);
#pragma unroll
      for (int c = 0; c < 2; ++c)
#pragma unroll
        for (int i = 0; i < 4; ++i)
          v[s2][c][i] = __builtin_amdgcn_raw_buffer_load_b128(rP, l * 16, (unsigned)((src * 4 + w) * 24576 + (c * 3 + J) * 4096 + i * 1024), 16);
    }
    u16* orow = out + (size_t)(mt * 192 + 96 * mw + 32 * J + r32) * (2 * DM) + br * DM + nt * 128 + 64 * nw + 8 * h;
#pragma unroll
    for (int c = 0; c < 2; ++c) {
      asm volatile("" : "+a"(acc[c][J]));
      unsigned pk[4][2];
#pragma unroll
      for (int i = 0; i < 4; ++i) {
        const f32x4 own = {acc[c][J][4 * i], acc[c][J][4 * i + 1], acc[c][J][4 * i + 2], acc[c][J][4 * i + 3]};
        f32x4 t[3];
#pragma unroll
        for (int s3 = 0; s3 < 3; ++s3) t[s3] = (s3 == J) ? own : __builtin_bit_cast(f32x4, v[s3 - (s3 > J ? 1 : 0)][c][i]);
        const f32x4 sum = (t[0] + t[1]) + t[2];
        pk[i][0] = cvtpk(sum[0], sum[1]); pk[i][1] = cvtpk(sum[2], sum[3]);
      }
#pragma unroll
      for (int i = 0; i < 4; i += 2) {
        auto r0 = __builtin_amdgcn_permlane32_swap(pk[i][0], pk[i + 1][0], false, false);
        auto r1 = __builtin_amdgcn_permlane32_swap(pk[i][1], pk[i + 1][1], false, false);
        u32x4 o = {r0[0], r1[0], r0[1], r1[1]};
        *reinterpret_cast<u32x4*>(orow + 32 * c + 8 * i) = o;
      }
    }
  };
  if (sk == 0) fin(std::integral_constant<int, 0>{});
  else if (sk == 1) fin(std::integral_constant<int, 1>{});
  else fin(std::integral_constant<int, 2>{});
}

extern "C" void kernel_launch(void* const* d_in, const int* in_sizes, int n_in,
                              void* d_out, int out_size, void* d_ws, size_t ws_size,
                              hipStream_t stream) {
  const u16* xA = (const u16*)d_in[0]; const u16* kA = (const u16*)d_in[1]; const u16* vA = (const u16*)d_in[2];
  const u16* wqA = (const u16*)d_in[3]; const u16* woA = (const u16*)d_in[4];
  const u16* xB = (const u16*)d_in[5]; const u16* kB = (const u16*)d_in[6]; const u16* vB = (const u16*)d_in[7];
  const u16* wqB = (const u16*)d_in[8]; const u16* woB = (const u16*)d_in[9];
  u16* Og = (u16*)d_ws;
  float* part = (float*)((char*)d_ws + WS_PART_OFF);
  unsigned* cnt = (unsigned*)((char*)d_ws + WS_CNT_OFF);
  (void)hipFuncSetAttribute((const void*)k1_attn, hipFuncAttributeMaxDynamicSharedMemorySize, K1_LDS);
  (void)hipFuncSetAttribute((const void*)k2_oproj, hipFuncAttributeMaxDynamicSharedMemorySize, K2_LDS);
  hipLaunchKernelGGL(k1_attn, dim3(256), dim3(256), K1_LDS, stream, xA, kA, vA, wqA, xB, kB, vB, wqB, Og, cnt);
  hipLaunchKernelGGL(k2_oproj, dim3(240), dim3(320), K2_LDS, stream, Og, woA, woB, (u16*)d_out, part, cnt);
}
```
